```python
import torch

def _softmax(x, axis=-1):
    return torch.softmax(x, dim=axis)

def _gelu(x):
    return torch.nn.functional.gelu(x, approximate='tanh')

def _silu(x):
    return torch.nn.functional.silu(x)

def _sigmoid(x):
    return torch.sigmoid(x)

def _layernorm(x, axis=-1, eps=1e-05):
    m = x.mean(dim=axis, keepdim=True)
    v = x.var(dim=axis, keepdim=True, unbiased=False)
    return (x - m) / torch.sqrt(v + eps)

def _scatter_add(tgt, idx, src, axis=0):
    return tgt.clone().index_add_(axis, idx, src)

def _segment_sum(src, seg, n):
    out = torch.zeros((n,) + src.shape[1:], dtype=src.dtype)
    return out.index_add_(0, seg, src)

def _pad(x, pw):
    flat = tuple((v for pair in reversed(pw) for v in pair))
    return torch.nn.functional.pad(x, flat)
kernel_name = 'interleave_layers__memory_aug_recurrent_mem_tr__mixer_token_mlp__prompt'

def setup_inputs():

    _w = lambda a: (lambda t: t.to(torch.bfloat16) if t.is_floating_point() else t)(torch.as_tensor(a))

    def _A_setup():
        B, S, Dm = (1, 2048, 640)
        H, Dh = (32, 128)
        M, seg = (8, 32)
        seg = min(seg, S)
        S = S // seg * seg
        return {'x': _w((lambda *s: torch.randn(*s, dtype=torch.bfloat16) * 0.05)(B, S, Dm)), 'mem0': _w((lambda *s: torch.randn(*s, dtype=torch.bfloat16) * 0.2)(B, M, Dm)), 'Wq': _w((lambda *s: torch.randn(*s, dtype=torch.bfloat16) * 0.8)(Dm, H, Dh) * Dm ** (-0.5)), 'Wk': _w((lambda *s: torch.randn(*s, dtype=torch.bfloat16) * 0.8)(Dm, H, Dh) * Dm ** (-0.5)), 'Wv': _w((lambda *s: torch.randn(*s, dtype=torch.bfloat16) * 0.2)(Dm, H, Dh) * Dm ** (-0.5)), 'Wo': _w((lambda *s: torch.randn(*s, dtype=torch.bfloat16) * 0.2)(H, Dh, Dm) * (H * Dh) ** (-0.5)), 'seg_len': seg}
    _A_in = _A_setup()

    def _B_setup():
        B, S, D, F = (1, 2048, 640, 1728)
        return {'x': _w((lambda *s: torch.randn(*s, dtype=torch.bfloat16) * 0.05)(B, S, D)), 'w1': _w((lambda *s: torch.randn(*s, dtype=torch.bfloat16) * 0.2)(S, F) * S ** (-0.5)), 'b1': _w((lambda *s: torch.randn(*s, dtype=torch.bfloat16) * 3.2)(F) * 0.02), 'w2': _w((lambda *s: torch.randn(*s, dtype=torch.bfloat16) * 0.1)(F, S) * F ** (-0.5)), 'b2': _w((lambda *s: torch.randn(*s, dtype=torch.bfloat16) * 0.2)(S) * 0.02)}
    _B_in = _B_setup()
    return {'A_x': _A_in['x'], 'A_mem0': _A_in['mem0'], 'A_Wq': _A_in['Wq'], 'A_Wk': _A_in['Wk'], 'A_Wv': _A_in['Wv'], 'A_Wo': _A_in['Wo'], 'A_seg_len': _A_in['seg_len'], 'B_x': _B_in['x'], 'B_w1': _B_in['w1'], 'B_b1': _B_in['b1'], 'B_w2': _B_in['w2'], 'B_b2': _B_in['b2']}

def reference(A_x, A_mem0, A_Wq, A_Wk, A_Wv, A_Wo, A_seg_len, B_x, B_w1, B_b1, B_w2, B_b2):

    def _A_ref(x, mem0, Wq, Wk, Wv, Wo, seg_len):
        B, S, Dm = x.shape
        H, Dh = (Wq.shape[1], Wq.shape[2])
        M = mem0.shape[1]
        scale = Dh ** (-0.5)
        n_seg = S // seg_len
        L = M + seg_len
        idx = torch.arange(L)
        mask = (idx[:, None] < M) | (idx[:, None] >= idx[None, :])
        mem = mem0
        outs = []
        for s in range(n_seg):
            xs = x[:, s * seg_len:(s + 1) * seg_len, :].clone()
            h = torch.concatenate([mem, xs], axis=1)
            q = torch.einsum('btd,dhe->bhte', h, Wq)
            k = torch.einsum('btd,dhe->bhte', h, Wk)
            v = torch.einsum('btd,dhe->bhte', h, Wv)
            scores = torch.einsum('bhqe,bhke->bhqk', q, k) * scale
            scores = torch.where(mask[None, None], scores, -1000000000.0)
            p = _softmax(scores, axis=-1)
            o = torch.einsum('bhqk,bhke->bhqe', p, v)
            o = torch.einsum('bhte,hed->btd', o, Wo)
            mem = o[:, :M, :].clone()
            outs.append(o[:, M:, :])
        return torch.concatenate(outs, axis=1)

    def _B_ref(x, w1, b1, w2, b2):
        B, S, D = x.shape
        h = _layernorm(x, axis=-1)
        u = torch.einsum('bsd,sf->bdf', h, w1) + b1
        u = _gelu(u)
        y = torch.einsum('bdf,fs->bds', u, w2) + b2
        return (lambda x, *d: x.permute(*d))(y, 0, 2, 1)
    x = A_x
    x = x + _A_ref(_layernorm(x, axis=-1), A_mem0, A_Wq, A_Wk, A_Wv, A_Wo, A_seg_len)
    x = x + _B_ref(_layernorm(x, axis=-1), B_w1, B_b1, B_w2, B_b2)
    x = x + _A_ref(_layernorm(x, axis=-1), A_mem0, A_Wq, A_Wk, A_Wv, A_Wo, A_seg_len)
    x = x + _B_ref(_layernorm(x, axis=-1), B_w1, B_b1, B_w2, B_b2)
    return x
```

```cpp
#include <hip/hip_runtime.h>
#include <hip/hip_bf16.h>
#include <stdint.h>
namespace naive {
typedef unsigned short u16;
__device__ __forceinline__ float b2f(u16 x) { return __uint_as_float((unsigned)x << 16); }
__device__ __forceinline__ u16 f2b(float f) { unsigned u = __float_as_uint(f); return (u16)((u + 0x7FFF + ((u >> 16) & 1)) >> 16); }
__device__ __forceinline__ float rb(float f) { return b2f(f2b(f)); }
constexpr int S = 2048, D = 640, H = 32, DH = 128, M = 8, SEG = 32, HD = 4096, F = 1728;
__global__ void ln_k(const u16* x, u16* y, int rows) {
    int r = blockIdx.x; __shared__ float sh[64];
    int t = threadIdx.x; float s = 0;
    for (int i = t; i < D; i += 64) s += b2f(x[r * D + i]);
    sh[t] = s; __syncthreads(); float tot = 0; for (int i = 0; i < 64; ++i) tot += sh[i]; __syncthreads();
    float mean = tot / D; float q = 0;
    for (int i = t; i < D; i += 64) { float d = b2f(x[r * D + i]) - mean; q += d * d; }
    sh[t] = q; __syncthreads(); float qt = 0; for (int i = 0; i < 64; ++i) qt += sh[i];
    float m = rb(mean), v = rb(qt / D); float sd = rb(sqrtf(rb(v + 1e-5f)));
    for (int i = t; i < D; i += 64) y[r * D + i] = f2b(rb(b2f(x[r * D + i]) - m) / sd);
}
__global__ void gemm_k(const u16* A, int sam, int sak, const u16* B, int sbk, int sbn, u16* C, int scm, int scn, int Mm, int Nn, int K, const u16* bias, int act, const u16* res) {
    __shared__ float As[16][17], Bs[16][17];
    int tx = threadIdx.x & 15, ty = threadIdx.x >> 4; int m = blockIdx.y * 16 + ty, n = blockIdx.x * 16 + tx; float acc = 0;
    for (int k0 = 0; k0 < K; k0 += 16) {
        int am = blockIdx.y * 16 + ty; As[ty][tx] = am < Mm ? b2f(A[(size_t)am * sam + (size_t)(k0 + tx) * sak]) : 0.f;
        Bs[ty][tx] = n < Nn ? b2f(B[(size_t)(k0 + ty) * sbk + (size_t)n * sbn]) : 0.f;
        __syncthreads();
        for (int k = 0; k < 16; ++k) acc += As[ty][k] * Bs[k][tx];
        __syncthreads();
    }
    if (m < Mm && n < Nn) {
        float v = rb(acc);
        if (bias) v = rb(v + b2f(bias[n]));
        if (act) { float u = v; v = rb(0.5f * u * (1.f + tanhf(0.7978845608028654f * (u + 0.044715f * u * u * u)))); }
        if (res) v = rb(v + b2f(res[(size_t)m * scm + (size_t)n * scn]));
        C[(size_t)m * scm + (size_t)n * scn] = f2b(v);
    }
}
__global__ void attn_k(const u16* qm, const u16* km, const u16* vm, const u16* qx, const u16* kx, const u16* vx, u16* om, u16* ox, int r0, int r1) {
    int h = blockIdx.x, r = r0 + blockIdx.y, t = threadIdx.x; __shared__ float p[40];
    const u16* q = (r < M ? qm + r * HD : qx + (r - M) * HD) + h * DH;
    if (t < 40) { const u16* k = (t < M ? km + t * HD : kx + (t - M) * HD) + h * DH; float s = 0; for (int e = 0; e < DH; ++e) s += b2f(q[e]) * b2f(k[e]);
        s = rb(rb(s) * rb(0.08838834764831845f)); if (!(r < M || r >= t)) s = rb(-1e9f); p[t] = s; }
    __syncthreads();
    float mx = -INFINITY; for (int j = 0; j < 40; ++j) mx = fmaxf(mx, p[j]); float sum = 0; for (int j = 0; j < 40; ++j) sum += expf(p[j] - mx);
    float o = 0; for (int j = 0; j < 40; ++j) { const u16* v = (j < M ? vm + j * HD : vx + (j - M) * HD) + h * DH; o += rb(expf(p[j] - mx) / sum) * b2f(v[t]); }
    (r < M ? om + r * HD : ox + (r - M) * HD)[h * DH + t] = f2b(o);
}
inline void gemm(hipStream_t st, const u16* A, int sam, int sak, const u16* B, int sbk, int sbn, u16* C, int scm, int scn, int Mm, int Nn, int K, const u16* bias = nullptr, int act = 0, const u16* res = nullptr) {
    gemm_k<<<dim3((Nn + 15) / 16, (Mm + 15) / 16), 256, 0, st>>>(A, sam, sak, B, sbk, sbn, C, scm, scn, Mm, Nn, K, bias, act, res);
}
struct Bufs { u16 *h, *q, *k, *v, *o, *mem, *qm, *km, *vm, *om, *x1, *h2, *u, *xa; };
inline void run(void* const* d_in, void* d_out, void* d_ws, hipStream_t st) {
    const u16* x = (const u16*)d_in[0]; const u16* mem0 = (const u16*)d_in[1];
    const u16 *Wq = (const u16*)d_in[2], *Wk = (const u16*)d_in[3], *Wv = (const u16*)d_in[4], *Wo = (const u16*)d_in[5];
    const u16 *w1 = (const u16*)d_in[8], *b1 = (const u16*)d_in[9], *w2 = (const u16*)d_in[10], *b2 = (const u16*)d_in[11];
    u16* w = (u16*)d_ws; size_t off = 0; auto al = [&](size_t n) { u16* p = w + off; off += (n + 127) / 128 * 128; return p; };
    u16 *h = al(S * D), *q = al((size_t)S * HD), *k = al((size_t)S * HD), *v = al((size_t)S * HD), *o = al((size_t)S * HD);
    u16 *mem = al(65 * M * D), *qm = al(M * HD), *km = al(M * HD), *vm = al(M * HD), *om = al(M * HD), *x1 = al(S * D), *h2 = al(S * D), *h3 = al(S * D), *u = al(D * F), *xa = al(S * D);
    const u16* cur = x;
    for (int layer = 0; layer < 2; ++layer) {
        ln_k<<<S, 64, 0, st>>>(cur, h, S);
        gemm(st, h, D, 1, Wq, HD, 1, q, HD, 1, S, HD, D); gemm(st, h, D, 1, Wk, HD, 1, k, HD, 1, S, HD, D); gemm(st, h, D, 1, Wv, HD, 1, v, HD, 1, S, HD, D);
        hipMemcpyAsync(mem, mem0, M * D * 2, hipMemcpyDeviceToDevice, st);
        for (int s = 0; s < 64; ++s) {
            const u16* mc = mem + s * M * D;
            gemm(st, mc, D, 1, Wq, HD, 1, qm, HD, 1, M, HD, D); gemm(st, mc, D, 1, Wk, HD, 1, km, HD, 1, M, HD, D); gemm(st, mc, D, 1, Wv, HD, 1, vm, HD, 1, M, HD, D);
            attn_k<<<dim3(H, 40), 128, 0, st>>>(qm, km, vm, q + (size_t)s * SEG * HD, k + (size_t)s * SEG * HD, v + (size_t)s * SEG * HD, om, o + (size_t)s * SEG * HD, 0, 40);
            gemm(st, om, HD, 1, Wo, D, 1, mem + (s + 1) * M * D, D, 1, M, D, HD);
        }
        gemm(st, o, HD, 1, Wo, D, 1, x1, D, 1, S, D, HD, nullptr, 0, cur);
        ln_k<<<S, 64, 0, st>>>(x1, h2, S); ln_k<<<S, 64, 0, st>>>(h2, h3, S);
        gemm(st, h3, 1, D, w1, F, 1, u, F, 1, D, F, S, b1, 1);
        u16* dst = layer == 0 ? xa : (u16*)d_out;
        gemm(st, u, F, 1, w2, S, 1, dst, 1, D, D, S, F, b2, 0, x1);
        cur = dst;
    }
}
}
extern "C" void kernel_launch(void* const* d_in, const int* in_sizes, int n_in, void* d_out, int out_size, void* d_ws, size_t ws_size, hipStream_t stream) {
    naive::run(d_in, d_out, d_ws, stream);
}
```

```cpp
#include <hip/hip_runtime.h>
#include <stdint.h>

#define DI __device__ __forceinline__
typedef unsigned short u16;
typedef __bf16 bf16x2 __attribute__((ext_vector_type(2)));
typedef __bf16 bf16x8 __attribute__((ext_vector_type(8)));
typedef __bf16 bf16x16 __attribute__((ext_vector_type(16)));
typedef short s16x4 __attribute__((ext_vector_type(4)));
typedef float f32x2 __attribute__((ext_vector_type(2)));
typedef float f32x4 __attribute__((ext_vector_type(4)));
typedef float f32x16 __attribute__((ext_vector_type(16)));
typedef unsigned u32x2 __attribute__((ext_vector_type(2)));
typedef unsigned u32x4 __attribute__((ext_vector_type(4)));
typedef unsigned u32x8 __attribute__((ext_vector_type(8)));
#define LAS __attribute__((address_space(3)))

constexpr int S = 2048, D = 640, H = 32, HD = 4096, F = 1728, NSEG = 64;
constexpr size_t MB = 1u << 20;
constexpr size_t WS_COMM = 0;
constexpr size_t WS_CTRL = 4 * MB;
constexpr size_t WS_PROG = WS_CTRL + 16384;
constexpr size_t WS_HF = 5 * MB;
constexpr size_t WS_X1 = 8 * MB;
constexpr size_t WS_X2 = 11 * MB;
constexpr size_t WS_H3 = 14 * MB;
constexpr size_t WS_U = 17 * MB;
constexpr size_t WS_QF = 20 * MB;
constexpr size_t WS_KF = 37 * MB;
constexpr size_t WS_BF = 54 * MB;
constexpr size_t WS_UX = 80 * MB;
constexpr size_t WS_PX = 164 * MB;
constexpr size_t WS_UM = 168 * MB;
constexpr size_t WS_END = 190 * MB;
#define PRING 8
constexpr unsigned C_MEM = 0, C_P = 655360, C_FH = C_P + PRING * 32768, C_END = C_FH + 4096;
constexpr int CT_QK = 0, CT_VU = 64  , CT_ABORT = 224, CT_NEAR = 240, CT_STAT = 256;
constexpr unsigned RETRY_CAP = 1u << 14;
#define CU 98
#define CH 88
#define XSLEEP 16
#define BACKOFF 0
#define PF_SLEEP 16
#define DLY_M 1000

DI float b2f(u16 x) { return __uint_as_float((unsigned)x << 16); }
DI unsigned pk2(float lo, float hi) { f32x2 v = {lo, hi}; return __builtin_bit_cast(unsigned, __builtin_convertvector(v, bf16x2)); }
DI float rbf(float x) { return __uint_as_float(pk2(x, x) << 16); }
DI float blo(unsigned w) { return __uint_as_float(w << 16); }
DI float bhi(unsigned w) { return __uint_as_float(w & 0xffff0000u); }
DI unsigned cmap(unsigned off, unsigned nearbit) { return ((off >> 13) << 14) + (nearbit << 13) + (off & 8191u); }
DI u32x4 mk_srd(const void* p, unsigned bytes) { u32x4 r; r.x = (unsigned)(size_t)p; r.y = (unsigned)((size_t)p >> 32); r.z = bytes; r.w = 0x00020000u; return r; }
DI unsigned rfl(unsigned x) { return __builtin_amdgcn_readfirstlane(x); }
DI unsigned clk() { unsigned long long t; asm volatile("s_memtime %0\n\ts_waitcnt lgkmcnt(0)" : "=s"(t)::"memory"); return (unsigned)t; }
DI void wait_until(unsigned t0, unsigned d) { while (clk() - t0 < d) {} }
DI void barrier() { asm volatile("s_waitcnt lgkmcnt(0)\n\ts_barrier" ::: "memory"); }
#define SB() __builtin_amdgcn_sched_barrier(0)
#define VMCNT(n) asm volatile("s_waitcnt vmcnt(" #n ")" ::: "memory")
template <int OFF> DI void dma_c(u32x4 srd, unsigned voff, unsigned soff, unsigned lds) {
    asm volatile("s_mov_b32 m0, %0\n\ts_nop 0\n\tbuffer_load_dwordx4 %1, %2, %3 offen offset:%4 sc1 lds" ::"s"(lds), "v"(voff), "s"(srd), "s"(soff), "i"(OFF) : "memory");
}
template <int OFF> DI void dma_p(u32x4 srd, unsigned voff, unsigned soff, unsigned lds) {
    asm volatile("s_mov_b32 m0, %0\n\ts_nop 0\n\tbuffer_load_dwordx4 %1, %2, %3 offen offset:%4 lds" ::"s"(lds), "v"(voff), "s"(srd), "s"(soff), "i"(OFF) : "memory");
}
DI __amdgpu_buffer_rsrc_t mk_rs(const void* p, unsigned bytes) { return __builtin_amdgcn_make_buffer_rsrc((void*)p, 0, bytes, 0x00020000); }
DI void st16c(__amdgpu_buffer_rsrc_t r, u32x4 v, unsigned voff, unsigned soff) { __builtin_amdgcn_raw_buffer_store_b128(v, r, voff, soff, 16); }
DI void st8c(__amdgpu_buffer_rsrc_t r, u32x2 v, unsigned voff, unsigned soff) { __builtin_amdgcn_raw_buffer_store_b64(v, r, voff, soff, 16); }
DI void st4c(__amdgpu_buffer_rsrc_t r, unsigned v, unsigned voff, unsigned soff) { __builtin_amdgcn_raw_buffer_store_b32(v, r, voff, soff, 16); }
DI u32x4 ld16c(__amdgpu_buffer_rsrc_t r, unsigned voff, unsigned soff) { return __builtin_amdgcn_raw_buffer_load_b128(r, voff, soff, 16); }
DI unsigned ld4c(__amdgpu_buffer_rsrc_t r, unsigned voff, unsigned soff) { return __builtin_amdgcn_raw_buffer_load_b32(r, voff, soff, 16); }
DI u32x4 lds16(unsigned a) { return *(LAS u32x4*)a; }
DI u32x2 lds8(unsigned a) { return *(LAS u32x2*)a; }
DI void lds16w(unsigned a, u32x4 v) { *(LAS u32x4*)a = v; }
DI void lds4w(unsigned a, unsigned v) { *(LAS unsigned*)a = v; }
DI u32x2 ldstr(unsigned a) { return __builtin_bit_cast(u32x2, __builtin_amdgcn_ds_read_tr16_b64_v4i16((LAS s16x4*)a)); }
DI bf16x8 bf8(u32x4 v) { return __builtin_bit_cast(bf16x8, v); }
DI bf16x8 bf8(u32x2 a, u32x2 b) { u32x4 v = {a.x, a.y, b.x, b.y}; return __builtin_bit_cast(bf16x8, v); }
DI f32x4 mfma16(bf16x8 a, bf16x8 b, f32x4 c) { return __builtin_amdgcn_mfma_f32_16x16x32_bf16(a, b, c, 0, 0, 0); }
DI f32x4 mfma16k16(u32x2 a, u32x2 b, f32x4 c) { return mfma16(bf8(a, (u32x2){0, 0}), bf8(b, (u32x2){0, 0}), c); }
DI f32x16 mfma32(bf16x8 a, bf16x8 b, f32x16 c) { return __builtin_amdgcn_mfma_f32_32x32x16_bf16(a, b, c, 0, 0, 0); }
DI f32x4 smfma(u32x4 a, u32x8 b, f32x4 c, int idx) { return __builtin_amdgcn_smfmac_f32_16x16x64_bf16(bf8(a), __builtin_bit_cast(bf16x16, b), c, idx, 0, 0); }
DI float halfsum(float a, float b) { auto r = __builtin_amdgcn_permlane32_swap(__float_as_uint(a), __float_as_uint(b), false, false); return __uint_as_float(r[0]) + __uint_as_float(r[1]); }
DI float xg_max(float x) {
    auto s = __builtin_amdgcn_permlane16_swap(__float_as_uint(x), __float_as_uint(x), false, false); x = fmaxf(__uint_as_float(s[0]), __uint_as_float(s[1]));
    auto t = __builtin_amdgcn_permlane32_swap(__float_as_uint(x), __float_as_uint(x), false, false); return fmaxf(__uint_as_float(t[0]), __uint_as_float(t[1]));
}
DI float xg_sum(float x) {
    auto s = __builtin_amdgcn_permlane16_swap(__float_as_uint(x), __float_as_uint(x), false, false); x = __uint_as_float(s[0]) + __uint_as_float(s[1]);
    auto t = __builtin_amdgcn_permlane32_swap(__float_as_uint(x), __float_as_uint(x), false, false); return __uint_as_float(t[0]) + __uint_as_float(t[1]);
}
DI unsigned max3u(unsigned a, unsigned b, unsigned c) { return max(max(a, b), c); }

DI int sp_row(int gB, int a, int pos) { return 16 * (2 * (a >> 1) + (gB >> 1)) + 8 * (pos >> 1) + 2 * (2 * (gB & 1) + (a & 1)) + (pos & 1); }

template <int CTRL> DI float dppf(float x) { return __builtin_bit_cast(float, __builtin_amdgcn_mov_dpp(__builtin_bit_cast(int, x), CTRL, 0xf, 0xf, true)); }
DI float row16_sum(float x) { x += dppf<0xB1>(x); x += dppf<0x4E>(x); x += dppf<0x141>(x); x += dppf<0x128>(x); return x; }
DI void ln40(float (&v)[40]) {
    float s = 0;
#pragma unroll
    for (int i = 0; i < 40; ++i) s += v[i];
    const float mean = row16_sum(s) * (1.f / D);
    float q = 0;
#pragma unroll
    for (int i = 0; i < 40; ++i) { float d = v[i] - mean; q += d * d; }
    const float var = rbf(row16_sum(q) * (1.f / D)), m = rbf(mean), sd = rbf(sqrtf(rbf(var + 1e-5f)));
#pragma unroll
    for (int i = 0; i < 40; ++i) v[i] = rbf(rbf(v[i] - m) / sd);
}
DI void load_row40(const u16* x, int row, int l16, float (&v)[40]) {
#pragma unroll
    for (int i = 0; i < 5; ++i) {
        u32x4 w = *(const u32x4*)(x + (size_t)row * D + (l16 + 16 * i) * 8);
#pragma unroll
        for (int j = 0; j < 4; ++j) { v[8 * i + 2 * j] = blo(w[j]); v[8 * i + 2 * j + 1] = bhi(w[j]); }
    }
}
DI void ln_to_hf(const u16* x, char* hf, int blk) {
    const int t = threadIdx.x & 255, row = blk * 16 + (t >> 4), l16 = t & 15;
    float v[40]; load_row40(x, row, l16, v); ln40(v);
#pragma unroll
    for (int i = 0; i < 5; ++i) {
        const int p = l16 + 16 * i;
        u32x4 w = {pk2(v[8 * i], v[8 * i + 1]), pk2(v[8 * i + 2], v[8 * i + 3]), pk2(v[8 * i + 4], v[8 * i + 5]), pk2(v[8 * i + 6], v[8 * i + 7])};
        *(u32x4*)(hf + (size_t)(row >> 5) * 40960 + (p >> 1) * 1024 + ((p & 1) * 32 + (row & 31)) * 16) = w;
    }
}
__global__ void __launch_bounds__(256) k_ln1(const u16* x, char* ws) { ln_to_hf(x, ws + WS_HF, blockIdx.x); }

constexpr int PRE_FOLD = 320, PRE_LN = 64, PRE_INIT = 64;
__global__ void __launch_bounds__(512) k_pre(const u16* x, const u16* mem0, const u16* Wv, const u16* Wo, char* ws) {
    extern __shared__ __attribute__((aligned(16))) char smem[];
    const int b = blockIdx.x, t = threadIdx.x, lane = t & 63, w = rfl(t >> 6);
    if (b < PRE_FOLD) {
        const int h = b / 10, ksp = (b % 10) >> 1, ch = b & 1;
        const unsigned lb = (unsigned)(size_t)(LAS char*)smem;
        const u32x4 so = mk_srd(Wo, HD * D * 2);
#pragma unroll
        for (int i = 0; i < 10; ++i) {
            const unsigned by = (w * 10 + i) * 1024 + lane * 16;
            dma_p<0>(so, (h * 128 + by / 640) * 1280 + ch * 640 + by % 640, 0, lb + (w * 10 + i) * 1024);
        }
        const int rt = w & 3, cg = w >> 2, r = lane & 31, hf = lane >> 5;
        const int ks = ksp * 2 + (rt >> 1), T = rt & 1;
        const int d = 64 * ks + sp_row(2 * T + ((r >> 2) & 1), r >> 3, r & 3);
        u32x4 a[8];
#pragma unroll
        for (int k = 0; k < 8; ++k) a[k] = *(const u32x4*)(Wv + (size_t)d * HD + h * 128 + 16 * k + 8 * hf);
        VMCNT(0); barrier();
        const int q = (lane >> 2) & 3, p = lane & 3, c16 = (lane >> 4) & 1;
#pragma unroll
        for (int ct = 0; ct < 5; ++ct) {
            const int col0 = (cg * 5 + ct) * 32;
            f32x16 acc = {};
#pragma unroll
            for (int k = 0; k < 8; ++k) {
                const unsigned ad = lb + (16 * k + 8 * hf + q) * 640 + (col0 + 16 * c16 + 4 * p) * 2;
                acc = mfma32(bf8(a[k]), bf8(ldstr(ad), ldstr(ad + 4 * 640)), acc);
            }
            const int col = ch * 320 + col0 + r, u = col >> 3, gB = 2 * T + hf;
            char* dst = ws + WS_BF + ((size_t)((u * 8 + (h >> 2)) * 2 + ((h >> 1) & 1)) * 10 + ks) * 2048 + (gB * 16 + (h & 1) * 8 + (col & 7)) * 16;
            *(u32x4*)dst = (u32x4){pk2(acc[0], acc[1]), pk2(acc[2], acc[3]), pk2(acc[4], acc[5]), pk2(acc[6], acc[7])};
            *(u32x4*)(dst + 1024) = (u32x4){pk2(acc[8], acc[9]), pk2(acc[10], acc[11]), pk2(acc[12], acc[13]), pk2(acc[14], acc[15])};
        }
    } else if (b < PRE_FOLD + PRE_LN) {
        ln_to_hf(x, ws + WS_HF, (b - PRE_FOLD) * 2 + (t >> 8));
    } else if (b < PRE_FOLD + PRE_LN + PRE_INIT) {
        const int ib = b - PRE_FOLD - PRE_LN;
        for (unsigned i = ib * 512 + t; i < 2 * 2 * 65536; i += PRE_INIT * 512) {
            const unsigned layer = i >> 17, half = (i >> 16) & 1, off = (i & 65535) * 16;
            u32x4 v;
            if (off < 10240) {
                const unsigned u = off >> 7, m = (off >> 4) & 7;
                v = *(const u32x4*)(mem0 + m * D + u * 8);
                v.x = min(v.x, 0xFFFFFFFEu); v.y = min(v.y, 0xFFFFFFFEu); v.z = min(v.z, 0xFFFFFFFEu); v.w = min(v.w, 0xFFFFFFFEu);
            } else if (off < C_P) v = (u32x4){~0u, ~0u, ~0u, ~0u};
            else v = (u32x4){0, 0, 0, 0};
            if (off < C_END) *(u32x4*)(ws + WS_COMM + layer * 2 * MB + cmap(off, half)) = v;
        }
        if (ib == 0) { for (int i = t; i < 12288; i += 512) if (i >= 2048 || (i & 1023) != CT_NEAR) ((unsigned*)(ws + WS_CTRL))[i] = 0; }
    } else if (t == 0) {
        const unsigned xcc = (unsigned)__builtin_amdgcn_s_getreg(20 | (3 << 11)) & 0xfu;
        for (int layer = 0; layer < 2; ++layer) {
            unsigned best[2] = {~0u, ~0u};
            for (int r = 0; r < 6; ++r) {
                char* p = ws + WS_COMM + layer * 2 * MB + 2 * MB - 16384 + (r & 1) * 8192;
                unsigned v;
                asm volatile("global_store_dword %0, %1, off sc1\n\ts_waitcnt vmcnt(0)" ::"v"(p), "v"(r) : "memory");
                const unsigned t0 = clk();
                asm volatile("global_load_dword %0, %1, off sc1\n\ts_waitcnt vmcnt(0)" : "=&v"(v) : "v"(p) : "memory");
                best[r & 1] = min(best[r & 1], clk() - t0);
            }
            ((unsigned*)(ws + WS_CTRL))[layer * 1024 + CT_NEAR] = (best[0] < best[1] ? 0u : 1u) ^ (xcc >= 4 ? 1u : 0u);
        }
    }
}

constexpr float SC_LOG2E = 0.08838834764831845f * 1.4426950408889634f, NEG = -1e30f;
DI float mx3(float a, float b, float c) { return __builtin_fmaxf(__builtin_fmaxf(a, b), c); }
DI f32x2 lo2(f32x4 v) { return (f32x2){v[0], v[1]}; }
DI f32x2 hi2(f32x4 v) { return (f32x2){v[2], v[3]}; }
DI f32x2 ex2(f32x2 v) { return (f32x2){__builtin_amdgcn_exp2f(v[0]), __builtin_amdgcn_exp2f(v[1])}; }
DI unsigned pk2v(f32x2 v) { return __builtin_bit_cast(unsigned, __builtin_convertvector(v, bf16x2)); }
DI void softmax40(f32x4 sx0, f32x4 sx1, f32x4 sm, u32x4& px, u32x2& pm) {
    float mx = mx3(mx3(mx3(sx0[0], sx0[1], sx0[2]), sx0[3], sx1[0]), mx3(sx1[1], sx1[2], sx1[3]), mx3(mx3(sm[0], sm[1], sm[2]), sm[3], NEG));
    { auto r = __builtin_amdgcn_permlane16_swap(__float_as_uint(mx), __float_as_uint(mx), false, false); mx = mx3(__uint_as_float(r[0]), __uint_as_float(r[1]), NEG); }
    { auto r = __builtin_amdgcn_permlane32_swap(__float_as_uint(mx), __float_as_uint(mx), false, false); mx = mx3(__uint_as_float(r[0]), __uint_as_float(r[1]), NEG); }
    mx *= SC_LOG2E;
    const f32x2 c = {SC_LOG2E, SC_LOG2E}, m = {mx, mx};
    const f32x2 e0 = ex2(lo2(sx0) * c - m), e1 = ex2(hi2(sx0) * c - m), e2 = ex2(lo2(sx1) * c - m), e3 = ex2(hi2(sx1) * c - m), e4 = ex2(lo2(sm) * c - m), e5 = ex2(hi2(sm) * c - m);
    const f32x2 t = ((e0 + e1) + (e2 + e3)) + (e4 + e5);
    const float inv = __builtin_amdgcn_rcpf(xg_sum(t[0] + t[1]));
    const f32x2 iv = {inv, inv};
    px = (u32x4){pk2v(e0 * iv), pk2v(e1 * iv), pk2v(e2 * iv), pk2v(e3 * iv)};
    pm = (u32x2){pk2v(e4 * iv), pk2v(e5 * iv)};
}

DI f32x2 halfsum2(f32x4 d) {
    auto a = __builtin_amdgcn_permlane32_swap(__float_as_uint(d[0]), __float_as_uint(d[2]), false, false);
    auto b = __builtin_amdgcn_permlane32_swap(__float_as_uint(d[1]), __float_as_uint(d[3]), false, false);
    return (f32x2){__uint_as_float(a[0]), __uint_as_float(b[0])} + (f32x2){__uint_as_float(a[1]), __uint_as_float(b[1])};
}
#define ROW16_STEP(op, c) op "_dpp %0, %0, %0 " c " row_mask:0xf bank_mask:0xf\n\t" op "_dpp %1, %1, %1 " c " row_mask:0xf bank_mask:0xf\n\ts_nop 0\n\t"
#define ROW16(op, a, b) asm("s_nop 1\n\t" ROW16_STEP(op, "quad_perm:[1,0,3,2]") ROW16_STEP(op, "quad_perm:[2,3,0,1]") ROW16_STEP(op, "row_half_mirror") ROW16_STEP(op, "row_mirror") : "+v"(a), "+v"(b))

DI unsigned rtc() { unsigned long long t; asm volatile("s_memrealtime %0\n\ts_waitcnt lgkmcnt(0)" : "=s"(t)::"memory"); return (unsigned)t; }
#define TR(i)
#define TRDUMP()
#define PHDECL
#define PH(i)
#define PHOUT(cond, base, n)
DI unsigned ux_slot(int u, int s) { return (u * 64 + ((s + 5 * u) & 63)) * 16384; }
DI unsigned qk_slot(int h, int s) { return (h * 64 + ((s + 3 * h) & 63)) * 8192; }
struct Ctx { const u16* xin; const u16 *Wq, *Wk, *Wv, *Wo; char* ws; int layer; };

template <int SLEEP = 2> DI void wait_prog(__amdgpu_buffer_rsrc_t rc, unsigned voff, unsigned need, unsigned& have, unsigned ctl) {
    for (unsigned n = 0; have < need; ++n) {
        asm volatile("" ::: "memory");
        const unsigned v = ld4c(rc, voff, 0);
        unsigned lo = 0;
        for (unsigned c = need; c <= 64 && __builtin_amdgcn_ballot_w64(v < c) == 0; c += 8) lo = c;
        have = lo;
        if (n > RETRY_CAP * 4) { st4c(rc, 1, ctl + CT_ABORT * 4, 0); have = 64; }
        if (lo == 0) __builtin_amdgcn_s_sleep(SLEEP);
    }
}
DI u32x2 ld_stamp(__amdgpu_buffer_rsrc_t rc, unsigned voff) { asm volatile("" ::: "memory"); return __builtin_amdgcn_raw_buffer_load_b64(rc, voff, 0, 16); }
DI unsigned newest_stamp(__amdgpu_buffer_rsrc_t rc, unsigned voff, unsigned want, unsigned& alive, unsigned lds) {
    unsigned now = rtc();
    u32x2 v = ld_stamp(rc, voff);
    for (unsigned n = 0; alive && (unsigned)__builtin_amdgcn_ballot_w64(v.x < want) != 0; ++n) {
        if (n > RETRY_CAP * 4) alive = 0;
        v = ld_stamp(rc, voff);
    }
    int d = (int)(v.y - now);
    d = max(d, __builtin_amdgcn_mov_dpp(d, 0xB1, 0xf, 0xf, true)); d = max(d, __builtin_amdgcn_mov_dpp(d, 0x4E, 0xf, 0xf, true));
    d = max(d, __builtin_amdgcn_mov_dpp(d, 0x141, 0xf, 0xf, true)); d = max(d, __builtin_amdgcn_mov_dpp(d, 0x140, 0xf, 0xf, true));
    if (alive) now += max(__builtin_amdgcn_readlane(d, 0), __builtin_amdgcn_readlane(d, 16));
    *(volatile LAS u32x2*)lds = (u32x2){want, now};
    return now;
}
DI unsigned lds_stamp(unsigned want, unsigned lds) {
    for (;;) { const u32x2 v = *(volatile LAS u32x2*)lds; if (rfl(v.x) == want) return rfl(v.y); }
}
DI void wait_rt(unsigned target) { while ((int)(rtc() - target) < 0) {} }

DI void role_head(const Ctx& c, int h, unsigned lb) {
    const int t = threadIdx.x, lane = t & 63, w = rfl(t >> 6), g = lane >> 4, col = lane & 15;
    constexpr unsigned L_MEM = 0, L_KQ = 10240  , L_QI = L_KQ + 32768, L_KI = L_QI + 2048, L_T = L_KI + 2048;
    const u32x4 sw = mk_srd(c.ws, WS_END);
    const __amdgpu_buffer_rsrc_t rw = mk_rs(c.ws, WS_END);
    const unsigned ctl = WS_CTRL + c.layer * 4096, prog = WS_PROG + c.layer * 16384, comm = WS_COMM + c.layer * 2 * MB, l16 = lane * 16;
    unsigned nearbit = rfl(*(const unsigned*)(c.ws + ctl + CT_NEAR * 4)); asm volatile("" : "+s"(nearbit));
    u32x8 wq[10], wk[10];
    {
        const u32x4 sq = mk_srd(c.Wq, D * HD * 2), sk = mk_srd(c.Wk, D * HD * 2);
        const unsigned vo = (w * 4 + (lane >> 4)) * 8192 + h * 256 + (lane & 15) * 16;
        auto stage = [&](int st) {
            const unsigned l0 = lb + (st & 1) * 65536;
#pragma unroll
            for (int i = 0; i < 4; ++i) { dma_p<0>(sq, vo, (st * 128 + i * 32) * 8192, l0 + i * 8192 + w * 1024); dma_p<0>(sk, vo, (st * 128 + i * 32) * 8192, l0 + 32768 + i * 8192 + w * 1024); }
        };
        stage(0);
#pragma unroll
        for (int st = 0; st < 5; ++st) {
            if (st < 4) { stage(st + 1); VMCNT(8); } else VMCNT(0);
            barrier();
#pragma unroll
            for (int k2 = 0; k2 < 2; ++k2)
#pragma unroll
                for (int a = 0; a < 4; ++a) {
                    const unsigned ad = lb + (st & 1) * 65536 + (k2 * 64 + sp_row(g, a, (lane >> 2) & 3)) * 256 + w * 32 + (lane & 3) * 8;
                    const u32x2 x = ldstr(ad), y = ldstr(ad + 32768);
                    wq[st * 2 + k2][2 * a] = x.x; wq[st * 2 + k2][2 * a + 1] = x.y; wk[st * 2 + k2][2 * a] = y.x; wk[st * 2 + k2][2 * a + 1] = y.y;
                }
            barrier();
        }
    }
    const int idx = (lane & 8) ? 0xEEEE : 0x4444;
    unsigned have = 0, nretry = 0, alive = ~0u;
    auto prefetch = [&](int s) {
        const unsigned src = ((w & 2) ? WS_QF : WS_KF) + qk_slot(h, s) + (w & 1) * 4096, dst = lb + L_KQ + (s & 1) * 16384 + (w - 4) * 4096;
        dma_c<0>(sw, l16, src, dst); dma_c<1024>(sw, l16, src, dst); dma_c<2048>(sw, l16, src, dst); dma_c<3072>(sw, l16, src, dst);
    };
    if (w >= 4) { wait_prog(rw, ctl + (CT_QK + h) * 4, 1, have, ctl); prefetch(0); wait_prog(rw, ctl + (CT_QK + h) * 4, 2, have, ctl); }
    const unsigned tra = (16 * (g >> 1) + 4 * (g & 1) + ((lane >> 2) & 3)) * 16 + (lane & 1) * 8;
    const unsigned ia = (w * 16 + col) * 16 + (4 * (g & 1) + 2 * (g >> 1)) * 2;
    const unsigned fh = comm + cmap(C_FH, nearbit);
    unsigned vf = lane < 32 ? lane * 128 : 0x80000000u; asm volatile("" : "+v"(vf)); vf += fh;
    if (t == 0) lds4w(lb + L_T, 0);
    const unsigned trq = (32 * (g >> 1) + 16 * (g & 1) + 8 * ((lane >> 3) & 1) + ((lane >> 2) & 1) + (lane & 2)) * 16 + (lane & 1) * 8;
    const unsigned po = (h & 3) * 640 + col * 16 + (2 * (g & 1) + (g >> 1)) * 4, pm_o = col < 8 ? po + 512 : 0x80000000u;
    const f32x2 mneg = col < 8 ? (f32x2){0.f, 0.f} : (f32x2){NEG, NEG};
    unsigned vmask = 0x7fff7fffu; asm volatile("" : "+v"(vmask));
    PHDECL;
    for (int s = 0; s < NSEG; ++s) {
        TR(0);
        if (w < 4) {
            const unsigned m0 = comm + cmap(C_MEM + s * 10240 + w * 1024, nearbit), m1 = comm + cmap(C_MEM + s * 10240 + (4 + w) * 1024, nearbit), m2 = comm + cmap(C_MEM + s * 10240 + (8 + (w & 1)) * 1024, nearbit);
            const unsigned ml = lb + L_MEM + w * 1024;
            if (s) wait_rt((w == 3 ? newest_stamp(rw, vf, s, alive, lb + L_T) : lds_stamp(s, lb + L_T)) + CU + CH);
            TR(4);
            for (;;) {
                dma_c<0>(sw, l16, m0, ml); dma_c<0>(sw, l16, m1, ml + 4096);
                if (w < 2) dma_c<0>(sw, l16, m2, ml + 8192);
                VMCNT(0);
                const u32x4 c0 = lds16(ml + l16), c1 = lds16(ml + 4096 + l16), c2 = lds16(ml + (w < 2 ? 8192 : 0) + l16);
                const unsigned mx = max3u(max3u(max3u(c0.x, c0.y, c0.z), c0.w, c1.x), max3u(max3u(c1.y, c1.z, c1.w), c2.x, c2.y), max(c2.z, c2.w));
                if (__builtin_expect(__builtin_amdgcn_ballot_w64(mx == ~0u) == 0 || nretry > RETRY_CAP, 1)) break;
                ++nretry;
                __builtin_amdgcn_s_sleep(BACKOFF);
            }
        } else VMCNT(0);
        TR(5); PH(0);
        const unsigned kq = lb + L_KQ + (s & 1) * 16384;
        barrier();
        TR(1); PH(1);
        if (t == 4 * 64) st8c(rw, (u32x2){(unsigned)s + 1, rtc()}, 0, fh + h * 128);
        u32x4 a[10];
#pragma unroll
        for (int k = 0; k < 10; ++k) a[k] = lds16(lb + L_MEM + k * 1024 + l16);
        SB();
        if (w >= 4 && s + 1 < NSEG) prefetch(s + 1);
        f32x4 q = {0, 0, 0, 0}, kk = q;
        SB();
#pragma unroll
        for (int k = 0; k < 10; ++k) { q = smfma(a[k], wq[k], q, idx); SB(); kk = smfma(a[k], wk[k], kk, idx); SB(); }
        PH(2);
        u32x8 kx[4];
        if (w < 3) {
#pragma unroll
            for (int k = 0; k < 4; ++k) { const u32x4 x = lds16(kq + l16 + k * 2048), y = lds16(kq + l16 + k * 2048 + 1024); kx[k] = (u32x8){x.x, x.y, x.z, x.w, y.x, y.y, y.z, y.w}; }
        } else {
#pragma unroll
            for (int k = 0; k < 4; ++k) asm volatile("" : "=v"(kx[k]));
        }
        lds4w(lb + L_QI + ia, pk2v(halfsum2(q)));
        lds4w(lb + L_KI + ia, pk2v(halfsum2(kk)));
        PH(3);
        barrier();
        TR(2); PH(4);
        if (w == 0) {
            u32x2 qr[4], kr[8];
#pragma unroll
            for (int k = 0; k < 4; ++k) qr[k] = ldstr(lb + L_QI + (k >> 1) * 1024 + (k & 1) * 64 + trq);
#pragma unroll
            for (int k = 0; k < 8; ++k) kr[k] = ldstr(lb + L_KI + (k >> 1) * 512 + (k & 1) * 128 + tra);
            SB();
            const u32x4 qa0 = {qr[0].x, qr[0].y, qr[1].x, qr[1].y}, qa1 = {qr[2].x, qr[2].y, qr[3].x, qr[3].y};
            f32x4 d0 = {0, 0, 0, 0}, d1 = d0, dm = d0;
            d0 = smfma(qa0, kx[0], d0, idx); d1 = smfma(qa0, kx[2], d1, idx);
            d0 = smfma(qa1, kx[1], d0, idx); d1 = smfma(qa1, kx[3], d1, idx);
            dm = smfma(qa0, (u32x8){kr[0].x, kr[0].y, kr[1].x, kr[1].y, kr[2].x, kr[2].y, kr[3].x, kr[3].y}, dm, idx);
            dm = smfma(qa1, (u32x8){kr[4].x, kr[4].y, kr[5].x, kr[5].y, kr[6].x, kr[6].y, kr[7].x, kr[7].y}, dm, idx);
            const f32x2 s0 = halfsum2(d0), s1 = halfsum2(d1), sm = halfsum2(dm) + mneg;
            float mxa = mx3(s0[0], s1[0], sm[0]), mxb = mx3(s0[1], s1[1], sm[1]);
            ROW16("v_max_f32", mxa, mxb);
            const f32x2 cc = {SC_LOG2E, SC_LOG2E}, mm = (f32x2){mxa, mxb} * cc;
            const f32x2 e0 = ex2(s0 * cc - mm), e1 = ex2(s1 * cc - mm), em = ex2(sm * cc - mm);
            const f32x2 tt = e0 + e1 + em;
            float sa = tt[0], sb = tt[1];
            ROW16("v_add_f32", sa, sb);
            const bool pos = (s / PRING) & 1;
            const float sgn = pos ? 1.f : -1.f;
            const f32x2 iv = (f32x2){__builtin_amdgcn_rcpf(sa), __builtin_amdgcn_rcpf(sb)} * (f32x2){sgn, sgn};
            const unsigned tag = pos ? 0u : 0x80008000u, pb = comm + cmap(C_P + (s % PRING) * 32768 + (h >> 2) * 4096, nearbit);
            if (s < NSEG - 1) {
                st4c(rw, (pk2v(e0 * iv) & vmask) | tag, po, pb); st4c(rw, (pk2v(e1 * iv) & vmask) | tag, po + 256, pb);
                st4c(rw, (pk2v(em * iv) & vmask) | tag, pm_o, pb);
            }
        } else if (w < 3) {
            const int qt = w - 1;
            f32x4 sx0 = {0, 0, 0, 0}, sx1 = sx0, sm = sx0;
#pragma unroll
            for (int k = 0; k < 4; ++k) {
                const bf16x8 qx = bf8(lds16(kq + 8192 + qt * 4096 + k * 1024 + l16));
                const u32x8 ka = kx[k >> 1], kb = kx[2 + (k >> 1)];
                sx0 = mfma16(bf8((k & 1) ? (u32x4){ka[4], ka[5], ka[6], ka[7]} : (u32x4){ka[0], ka[1], ka[2], ka[3]}), qx, sx0);
                sx1 = mfma16(bf8((k & 1) ? (u32x4){kb[4], kb[5], kb[6], kb[7]} : (u32x4){kb[0], kb[1], kb[2], kb[3]}), qx, sx1);
                sm = mfma16(bf8(ldstr(lb + L_KI + k * 512 + tra), ldstr(lb + L_KI + k * 512 + 128 + tra)), qx, sm);
            }
            const int lim = 16 * qt + col - (16 * (g >> 1) + 4 * (g & 1));
#pragma unroll
            for (int r = 0; r < 4; ++r) { if (r > lim) sx0[r] = NEG; if (r + 8 > lim) sx1[r] = NEG; }
            if (g >= 2) sm = (f32x4){NEG, NEG, NEG, NEG};
            u32x4 px; u32x2 pm;
            softmax40(sx0, sx1, sm, px, pm);
            const unsigned base = WS_PX + (s * 32 + h) * 2048;
            if (qt == 1 || g < 2) st16c(rw, px, (1 - qt) * 1024 + l16, base);
            if (g < 2) st8c(rw, pm, 1536 + qt * 256 + col * 16 + g * 8, base);
        } else if (w == 3) {
            __builtin_amdgcn_s_sleep(PF_SLEEP);
            if (lane == 0) st4c(rw, s, prog + h * 128, 0);
        } else if (have < s + 3 && s + 2 < NSEG) { __builtin_amdgcn_s_sleep(PF_SLEEP); wait_prog(rw, ctl + (CT_QK + h) * 4, s + 3, have, ctl); }
        TR(3); PH(5);
    }
    PHOUT(h == 5, 320, 56);
    VMCNT(0); barrier();
    if (t == 0) st4c(rw, NSEG, prog + h * 128, 0);
    if (lane == 0 && nretry > RETRY_CAP) st4c(rw, 1, ctl + CT_ABORT * 4, 0);
    TRDUMP();
}

DI void role_u(const Ctx& c, int u, unsigned lb) {
    const int t = threadIdx.x, lane = t & 63, w = rfl(t >> 6), g = lane >> 4, col = lane & 15;
    constexpr unsigned L_MEM = 0, L_P = 10240  , L_UX = L_P + 20480  , L_R = L_UX + 32768  , L_T = L_R + 5120, L_UM = L_T + 64  ;
    const u32x4 sw = mk_srd(c.ws, WS_END);
    const __amdgpu_buffer_rsrc_t rw = mk_rs(c.ws, WS_END);
    const unsigned ctl = WS_CTRL + c.layer * 4096, prog = WS_PROG + c.layer * 16384, comm = WS_COMM + c.layer * 2 * MB;
    unsigned nearbit = rfl(*(const unsigned*)(c.ws + ctl + CT_NEAR * 4)); asm volatile("" : "+s"(nearbit));
    u32x8 wb[2][10];
    {
        const char* src = c.ws + WS_BF + (size_t)(u * 8 + w) * 40960 + lane * 16;
#pragma unroll
        for (int tt = 0; tt < 2; ++tt)
#pragma unroll
            for (int k = 0; k < 10; ++k) {
                const u32x4 x = *(const u32x4*)(src + (tt * 10 + k) * 2048), y = *(const u32x4*)(src + (tt * 10 + k) * 2048 + 1024);
                wb[tt][k] = (u32x8){x.x, x.y, x.z, x.w, y.x, y.y, y.z, y.w};
            }
    }
    const unsigned pl = lb + L_P + w * 2560, l16 = lane * 16;
    const int idx = (lane & 8) ? 0xEEEE : 0x4444;
    unsigned have = 0, nr1 = 0, nr3 = 0, alive = ~0u;
    const unsigned pvo = ctl + (CT_VU + (u >= 40 ? 32 : 0) + (lane & 31)) * 4;
    const bool diag = (col >> 3) == (g >> 1);
    const unsigned ra = lb + L_R + ((col & 7) * 2 + (g & 1)) * 80 + (w * 2 + (g >> 1)) * 4;
    auto ux_dma = [&](int s) {
        const unsigned src = WS_UX + ux_slot(u, s) + (w - 6) * 8192, dst = lb + L_UX + (s & 1) * 16384 + (w - 6) * 8192;
        dma_c<0>(sw, l16, src, dst); dma_c<1024>(sw, l16, src, dst); dma_c<2048>(sw, l16, src, dst); dma_c<3072>(sw, l16, src, dst);
        dma_c<0>(sw, l16, src + 4096, dst + 4096); dma_c<1024>(sw, l16, src + 4096, dst + 4096); dma_c<2048>(sw, l16, src + 4096, dst + 4096); dma_c<3072>(sw, l16, src + 4096, dst + 4096);
    };
    auto um_store = [&](int s) {
        const unsigned o = (w - 6) * 2048 + l16;
        const unsigned l = lb + L_UM + (s & 1) * 4096 + o;
        st16c(rw, lds16(l), o, WS_UM + ux_slot(u, s) / 4); st16c(rw, lds16(l + 1024), o + 1024, WS_UM + ux_slot(u, s) / 4);
    };
    u32x4 ux0, ux1;
    u32x2 um;
    unsigned vf = lane < 32 ? lane * 128 : 0x80000000u; asm volatile("" : "+v"(vf)); vf += comm + cmap(C_FH, nearbit);
    if (t == 0) lds4w(lb + L_T, 0);
    PHDECL;
    auto um_calc = [&](int s) {
        if (w < 5) {
            const unsigned ml = lb + L_MEM + w * 1024, m0 = comm + cmap(C_MEM + s * 10240 + w * 1024, nearbit), m1 = comm + cmap(C_MEM + s * 10240 + (w + 5) * 1024, nearbit);
            for (;;) {
                dma_c<0>(sw, l16, m0, ml); dma_c<0>(sw, l16, m1, ml + 5120);
                VMCNT(0);
                const u32x4 c0 = lds16(ml + l16), c1 = lds16(ml + 5120 + l16);
                if (__builtin_amdgcn_ballot_w64(max3u(max3u(max3u(c0.x, c0.y, c0.z), c0.w, c1.x), max3u(c1.y, c1.z, c1.w), 0) == ~0u) == 0 || nr3 > RETRY_CAP) break;
                ++nr3;
                __builtin_amdgcn_s_sleep(BACKOFF);
            }
        }
        else { if (w >= 6 && s + 1 < NSEG - 1) wait_prog(rw, pvo, s + 2, have, ctl); VMCNT(0); }
        PH(5); if (s > 3) { --s; TR(6); ++s; }
        barrier();
        PH(6); if (s > 3) { --s; TR(7); ++s; }
        f32x4 e0 = {0, 0, 0, 0}, e1 = e0;
        u32x4 a[10];
#pragma unroll
        for (int k = 0; k < 10; ++k) a[k] = lds16(lb + L_MEM + k * 1024 + l16);
        SB();
#pragma unroll
        for (int k = 0; k < 10; ++k) { e0 = smfma(a[k], wb[0][k], e0, idx); SB(); e1 = smfma(a[k], wb[1][k], e1, idx); SB(); }
        ux0 = lds16(lb + L_UX + (s & 1) * 16384 + w * 2048 + l16); ux1 = lds16(lb + L_UX + (s & 1) * 16384 + w * 2048 + 1024 + l16);
        um = (u32x2){pk2(halfsum(e0[0], e1[0]), halfsum(e0[1], e1[1])), pk2(halfsum(e0[2], e1[2]), halfsum(e0[3], e1[3]))};
        *(LAS u32x2*)(lb + L_UM + (s & 1) * 4096 + w * 512 + ((2 * (g >> 1) + (col >> 3)) * 8 + (col & 7)) * 16 + (g & 1) * 8) = um;
    };
    if (w >= 6) { wait_prog(rw, pvo, 1, have, ctl); ux_dma(0); wait_prog(rw, pvo, 2, have, ctl); ux_dma(1); }
    um_calc(0);
    const unsigned pt = pl + ((lane >> 1) & 1) * 640 + (4 * g + ((lane >> 2) & 3)) * 16 + (lane & 1) * 8;
    const unsigned ptm = pl + ((g >> 1) * 2 + ((lane >> 1) & 1)) * 640 + 512 + (4 * (g & 1) + ((lane >> 2) & 3)) * 16 + (lane & 1) * 8;
    for (int s = 0; s < NSEG - 1; ++s) {
        TR(0);
        const unsigned psrc = comm + cmap(C_P + (s % PRING) * 32768 + w * 4096, nearbit);
        const unsigned go = (w == 3 ? newest_stamp(rw, vf, s + 1, alive, lb + L_T) : lds_stamp(s + 1, lb + L_T)) + CU;
        wait_rt(go);
        TR(4); PH(8);
        dma_c<0>(sw, l16, psrc, pl); dma_c<1024>(sw, l16, psrc, pl);
        if (lane < 32) dma_c<2048>(sw, l16, psrc, pl);
        if (w >= 6 && s > 0) {
            __builtin_amdgcn_s_sleep(2);
            if (s + 1 < NSEG - 1) ux_dma(s + 1);
            um_store(s - 1);
            st4c(rw, s - 1, t == 6 * 64 ? prog + (32 + u) * 128 : 0x80000000u, 0);
            if (s + 1 < NSEG - 1) VMCNT(11); else VMCNT(3);
        } else VMCNT(0);
        TR(1); PH(0);
        const bool pos = (s / PRING) & 1;
        const unsigned tag = pos ? 0u : 0x80008000u;
        for (;;) {
            const u32x2 p0 = ldstr(pt), p1 = ldstr(pt + 256), p2 = ldstr(pt + 1280), p3 = ldstr(pt + 1536), pm = ldstr(ptm);
            f32x4 acc = mfma16(bf8(ux0), bf8(p0, p1), (f32x4){0, 0, 0, 0});
            acc = mfma16(bf8(ux1), bf8(p2, p3), acc);
            acc = mfma16k16(um, pm, acc);
            const bool bad = ((((p0.x ^ tag) | (p0.y ^ tag)) | ((p1.x ^ tag) | (p1.y ^ tag)) | ((p2.x ^ tag) | (p2.y ^ tag)) | ((p3.x ^ tag) | (p3.y ^ tag)) | ((pm.x ^ tag) | (pm.y ^ tag))) & 0x80008000u) != 0;
            if (diag) {
#pragma unroll
                for (int r = 0; r < 4; ++r) lds4w(ra + r * 1280, __float_as_uint(acc[r]));
            }
            if (__builtin_expect(__builtin_amdgcn_ballot_w64(bad) == 0 || nr1 > RETRY_CAP, 1)) break;
            ++nr1;
            dma_c<0>(sw, l16, psrc, pl); dma_c<1024>(sw, l16, psrc, pl);
            if (lane < 32) dma_c<2048>(sw, l16, psrc, pl);
            VMCNT(0);
        }
        TR(5); PH(1);
        barrier();
        PH(2);
        if (w == 0) {
            const unsigned ad = lb + L_R + lane * 80;
            const f32x4 v = (__builtin_bit_cast(f32x4, lds16(ad)) + __builtin_bit_cast(f32x4, lds16(ad + 16))) + (__builtin_bit_cast(f32x4, lds16(ad + 32)) + __builtin_bit_cast(f32x4, lds16(ad + 48)));
            const f32x2 v2 = lo2(v) + hi2(v);
            float x = v2[0] + v2[1];
            if (!pos) x = -x;
            auto r = __builtin_amdgcn_permlane16_swap(__float_as_uint(x), __float_as_uint(x), false, false);
            const unsigned o = min(pk2(__uint_as_float(r[0]), __uint_as_float(r[1])), 0xFFFFFFFEu);
            if (!(g & 1)) st4c(rw, o, col * 8 + (g >> 1) * 4, comm + cmap(C_MEM + (s + 1) * 10240 + u * 128, nearbit));
        }
        TR(2); PH(3);
        if (w < 5) wait_rt(go + CH);
        PH(4);
        um_calc(s + 1);
        TR(3); PH(7);
    }
    PHOUT(u == 7, 400, 55);
    barrier();
    if (w >= 6) { um_store(NSEG - 2); um_store(NSEG - 1); }
    VMCNT(0); barrier();
    if (t == 0) st4c(rw, NSEG, prog + (32 + u) * 128, 0);
    if (lane == 0 && (nr1 > RETRY_CAP || nr3 > RETRY_CAP)) st4c(rw, 1, ctl + CT_ABORT * 4, 0);
    TRDUMP();
}

DI void role_x(const Ctx& c, int x, unsigned lb) {
    const int t = threadIdx.x, lane = t & 63, w = rfl(t >> 6), g = lane >> 4, col = lane & 15;
    constexpr unsigned WV = 13568, O_UX = 8192  , O_UM = O_UX + 4352, L_R = 8 * WV  , L_FAIL = L_R + 32768;
    const u32x4 sw = mk_srd(c.ws, WS_END);
    const __amdgpu_buffer_rsrc_t rw = mk_rs(c.ws, WS_END);
    const unsigned ctl = WS_CTRL + c.layer * 4096, wl = lb + w * WV, l16 = lane * 16;
    const unsigned pv1 = ctl + (CT_VU + (x >= 20 ? 32 : 0) + (lane & 31)) * 4, pv2 = WS_PROG + c.layer * 16384 + (lane < 32 ? lane : 32 + 2 * x + (lane & 1)) * 128;
    unsigned have1 = 0, have2 = 0;
    int x1 = rfl(2 * x + 1); asm volatile("" : "+s"(x1));
    for (int s = 0; s < NSEG; ++s) {
        TR(0);
        wait_prog(rw, pv1, s + 1, have1, ctl);
        {
            const unsigned src = WS_UX + ux_slot(2 * x, s) + w * 2048, src1 = WS_UX + ux_slot(x1, s) + w * 2048;
            dma_c<0>(sw, l16, src, wl + O_UX); dma_c<1024>(sw, l16, src, wl + O_UX);
            dma_c<0>(sw, l16, src1, wl + O_UX + 2176); dma_c<1024>(sw, l16, src1, wl + O_UX + 2176);
        }
        const size_t oo = (size_t)(s * 32 + (w & 1) * 16 + col) * D + x * 16 + g * 4;
        u32x2 xr = {0, 0};
        if (w < 2) xr = *(const u32x2*)(c.xin + oo);
        if (w == 7) wait_prog<XSLEEP>(rw, pv2, s + 1, have2, ctl);
        barrier();
        TR(1);
        {
            const unsigned src = WS_PX + (s * 32 + w * 4) * 2048;
            dma_c<0>(sw, l16, src, wl); dma_c<1024>(sw, l16, src, wl); dma_c<2048>(sw, l16, src, wl); dma_c<3072>(sw, l16, src, wl);
            dma_c<0>(sw, l16, src + 4096, wl + 4096); dma_c<1024>(sw, l16, src + 4096, wl + 4096); dma_c<2048>(sw, l16, src + 4096, wl + 4096); dma_c<3072>(sw, l16, src + 4096, wl + 4096);
            dma_c<0>(sw, ux_slot(2 * x + (lane >> 5), s) / 4 + (lane & 31) * 16, WS_UM + w * 512, wl + O_UM);
        }
        VMCNT(0);
        TR(2);
        f32x4 a0 = {0, 0, 0, 0}, a1 = a0;
        const unsigned ub = wl + O_UX + (col >> 3) * 2176 + (col & 7) * 16;
#pragma unroll
        for (int hh = 0; hh < 4; ++hh) a1 = mfma16(bf8(lds16(ub + (hh >> 1) * 1024 + g * 256 + (hh & 1) * 128)), bf8(lds16(wl + hh * 2048 + l16)), a1);
#pragma unroll
        for (int hp = 0; hp < 2; ++hp) a0 = mfma16(bf8(lds16(ub + hp * 1024 + (g & 1) * 256 + (g >> 1) * 128)), bf8(lds16(wl + (2 * hp + (g >> 1)) * 2048 + 1024 + (lane & 31) * 16)), a0);
        {
            const bf16x8 um = bf8(lds16(wl + O_UM + (col >> 3) * 512 + g * 128 + (col & 7) * 16));
            a0 = mfma16(um, bf8(lds16(wl + g * 2048 + 1536 + col * 16)), a0);
            a1 = mfma16(um, bf8(lds16(wl + g * 2048 + 1792 + col * 16)), a1);
        }
        const unsigned rb = lb + L_R + (s & 1) * 16384;
        lds16w(rb + w * 1024 + l16, __builtin_bit_cast(u32x4, a0));
        lds16w(rb + 8192 + w * 1024 + l16, __builtin_bit_cast(u32x4, a1));
        barrier();
        if (w < 2) {
            f32x4 v = {0, 0, 0, 0};
#pragma unroll
            for (int i = 0; i < 8; ++i) v += __builtin_bit_cast(f32x4, lds16(rb + w * 8192 + i * 1024 + l16));
            *(u32x2*)((u16*)(c.ws + WS_X1) + oo) = (u32x2){pk2(blo(xr.x) + rbf(v[0]), bhi(xr.x) + rbf(v[1])), pk2(blo(xr.y) + rbf(v[2]), bhi(xr.y) + rbf(v[3]))};
        }
        TR(3);
    }
    TRDUMP();
}

DI void h_dma(u32x4 sw, int s, int w, unsigned l16, unsigned lb) {
    const unsigned src = WS_HF + s * 40960 + w * 5120, dst = lb + (s % 3) * 40960 + w * 5120;
    dma_p<0>(sw, l16, src, dst); dma_p<1024>(sw, l16, src, dst); dma_p<2048>(sw, l16, src, dst); dma_p<3072>(sw, l16, src, dst);
    dma_p<0>(sw, l16, src + 4096, dst + 4096);
}
DI u32x4 pk8(const f32x16& a, int o) { return (u32x4){pk2(a[o], a[o + 1]), pk2(a[o + 2], a[o + 3]), pk2(a[o + 4], a[o + 5]), pk2(a[o + 6], a[o + 7])}; }

DI void role_qk(const Ctx& c, int h, unsigned lb) {
    const int t = threadIdx.x, lane = t & 63, w = rfl(t >> 6), r = lane & 31, hf = lane >> 5, et = w & 3, mat = w >> 2;
    const u32x4 sw = mk_srd(c.ws, WS_END);
    const __amdgpu_buffer_rsrc_t rw = mk_rs(c.ws, WS_END);
    const unsigned ctl = WS_CTRL + c.layer * 4096, l16 = lane * 16;
    u32x4 w1[40];
    {
        const u32x4 s1 = mk_srd(mat ? c.Wk : c.Wq, D * HD * 2);
        const unsigned vo = (lane >> 4) * 8192 + h * 256 + (lane & 15) * 16;
        auto stage = [&](int st) {
#pragma unroll
            for (int i = 0; i < 10; ++i) dma_p<0>(s1, vo, (st * 160 + (et * 10 + i) * 4) * 8192, lb + (st & 1) * 81920 + (w * 10 + i) * 1024);
        };
        stage(0);
#pragma unroll
        for (int st = 0; st < 4; ++st) {
            if (st < 3) { stage(st + 1); VMCNT(10); } else VMCNT(0);
            barrier();
#pragma unroll
            for (int k = 0; k < 10; ++k) {
                const unsigned ad = lb + (st & 1) * 81920 + (mat * 160 + 16 * k + 8 * hf + ((lane >> 2) & 3)) * 256 + (et * 32 + 16 * ((lane >> 4) & 1) + 4 * (lane & 3)) * 2;
                const u32x2 x = ldstr(ad), y = ldstr(ad + 1024);
                w1[st * 10 + k] = (u32x4){x.x, x.y, y.x, y.y};
            }
            barrier();
        }
    }
    const unsigned pr = ctl + (CT_QK + h) * 4;
    h_dma(sw, 0, w, l16, lb); h_dma(sw, 1, w, l16, lb); VMCNT(5); barrier();
    const unsigned qko = mat == 0 ? (r >> 4) * 4096 + et * 1024 + (hf * 16 + (r & 15)) * 16 : ((r >> 3) & 1) * 4096 + et * 1024 + (hf * 16 + 8 * (r >> 4) + 4 * ((r >> 2) & 1) + (r & 3)) * 16;
    for (int s = 0; s < NSEG; ++s) {
        TR(0);
        if (s + 2 < NSEG) h_dma(sw, s + 2, w, l16, lb);
        f32x16 acc = {};
        const unsigned hb = lb + (s % 3) * 40960 + l16;
        u32x4 b[8];
#pragma unroll
        for (int k = 0; k < 8; ++k) b[k] = lds16(hb + k * 1024);
        SB();
#pragma unroll
        for (int k = 0; k < 40; ++k) { acc = mfma32(bf8(w1[k]), bf8(b[k & 7]), acc); if (k + 8 < 40) b[k & 7] = lds16(hb + (k + 8) * 1024); SB(); }
        TR(1);
        if (s + 2 < NSEG) VMCNT(5); else VMCNT(0);
        barrier();
        if (t == 0 && s > 1) st4c(rw, s, pr, 0);
        const unsigned dst = (mat == 0 ? WS_QF : WS_KF) + qk_slot(h, s);
        st16c(rw, pk8(acc, 0), qko, dst); st16c(rw, pk8(acc, 8), qko + 512, dst);
        if (s == 0 || s == NSEG - 1) { VMCNT(0); barrier(); if (t == 0) st4c(rw, s + 1, pr, 0); }
    }
    TRDUMP();
}

DI void role_vu(const Ctx& c, int ch, int h, unsigned lb) {
    const int t = threadIdx.x, lane = t & 63, w = rfl(t >> 6), r = lane & 31, hf = lane >> 5, et = w & 3, kh = w >> 2;
    constexpr unsigned L_EX = 122880  , L_V = L_EX + 16384;
    const u32x4 sw = mk_srd(c.ws, WS_END);
    const __amdgpu_buffer_rsrc_t rw = mk_rs(c.ws, WS_END);
    const unsigned ctl = WS_CTRL + c.layer * 4096, l16 = lane * 16;
    const int q = (lane >> 2) & 3, p = lane & 3, c16 = (lane >> 4) & 1;
    u32x4 w1[20], w2[2][8];
    {
        const u32x4 s1 = mk_srd(c.Wv, D * HD * 2);
        const unsigned vo = (lane >> 4) * 8192 + h * 256 + (lane & 15) * 16;
#pragma unroll
        for (int st = 0; st < 2; ++st)
#pragma unroll
            for (int i = 0; i < 10; ++i) dma_p<0>(s1, vo, (kh * 320 + st * 160 + (et * 10 + i) * 4) * 8192, lb + st * 81920 + (w * 10 + i) * 1024);
#pragma unroll
        for (int st = 0; st < 2; ++st) {
            if (st == 0) VMCNT(10); else VMCNT(0);
            barrier();
#pragma unroll
            for (int k = 0; k < 10; ++k) {
                const unsigned ad = lb + st * 81920 + (kh * 160 + 16 * k + 8 * hf + q) * 256 + (et * 32 + 16 * c16 + 4 * p) * 2;
                const u32x2 x = ldstr(ad), y = ldstr(ad + 1024);
                w1[st * 10 + k] = (u32x4){x.x, x.y, y.x, y.y};
            }
            if (st == 0) {
                barrier();
                const u32x4 so = mk_srd(c.Wo, HD * D * 2);
#pragma unroll
                for (int i = 0; i < 10; ++i) {
                    const unsigned by = (w * 10 + i) * 1024 + l16;
                    dma_p<0>(so, (h * 128 + by / 640) * 1280 + ch * 640 + by % 640, 0, lb + (w * 10 + i) * 1024);
                }
            }
        }
#pragma unroll
        for (int tt = 0; tt < 2; ++tt)
#pragma unroll
            for (int k = 0; k < 8; ++k) {
                const int ct = tt == 0 ? w : 8 + (w & 1);
                const unsigned ad = lb + (16 * k + 4 * hf + q) * 640 + (ct * 32 + 16 * c16 + 4 * p) * 2;
                const u32x2 x = ldstr(ad), y = ldstr(ad + 8 * 640);
                w2[tt][k] = (u32x4){x.x, x.y, y.x, y.y};
            }
        barrier();
    }
    const unsigned pr = ctl + (CT_VU + ch * 32 + h) * 4;
    h_dma(sw, 0, w, l16, lb); h_dma(sw, 1, w, l16, lb); VMCNT(5); barrier();
    for (int s = 0; s < NSEG; ++s) {
        TR(0);
        if (s + 2 < NSEG) h_dma(sw, s + 2, w, l16, lb);
        f32x16 acc = {};
        const unsigned hb = lb + (s % 3) * 40960 + kh * 20480 + l16;
        u32x4 b[8];
#pragma unroll
        for (int k = 0; k < 8; ++k) b[k] = lds16(hb + k * 1024);
        SB();
#pragma unroll
        for (int k = 0; k < 20; ++k) { acc = mfma32(bf8(w1[k]), bf8(b[k & 7]), acc); if (k + 8 < 20) b[k & 7] = lds16(hb + (k + 8) * 1024); SB(); }
        const unsigned ex = lb + L_EX;
        f32x4 m0, m1;
        if (kh == 0) {
            lds16w(ex + w * 2048 + l16, __builtin_bit_cast(u32x4, (f32x4){acc[8], acc[9], acc[10], acc[11]}));
            lds16w(ex + w * 2048 + 1024 + l16, __builtin_bit_cast(u32x4, (f32x4){acc[12], acc[13], acc[14], acc[15]}));
            m0 = (f32x4){acc[0], acc[1], acc[2], acc[3]}; m1 = (f32x4){acc[4], acc[5], acc[6], acc[7]};
        } else {
            lds16w(ex + w * 2048 + l16, __builtin_bit_cast(u32x4, (f32x4){acc[0], acc[1], acc[2], acc[3]}));
            lds16w(ex + w * 2048 + 1024 + l16, __builtin_bit_cast(u32x4, (f32x4){acc[4], acc[5], acc[6], acc[7]}));
            m0 = (f32x4){acc[8], acc[9], acc[10], acc[11]}; m1 = (f32x4){acc[12], acc[13], acc[14], acc[15]};
        }
        TR(1);
        if (s + 2 < NSEG) VMCNT(5); else VMCNT(0);
        barrier();
        TR(2);
        if (t == 0 && s > 1) st4c(rw, s, pr, 0);
        m0 += __builtin_bit_cast(f32x4, lds16(ex + (w ^ 4) * 2048 + l16));
        m1 += __builtin_bit_cast(f32x4, lds16(ex + (w ^ 4) * 2048 + 1024 + l16));
        lds16w(lb + L_V + (2 * et + kh) * 1024 + l16, (u32x4){pk2(m0[0], m0[1]), pk2(m0[2], m0[3]), pk2(m1[0], m1[1]), pk2(m1[2], m1[3])});
        barrier();
        bf16x8 v[8];
#pragma unroll
        for (int k = 0; k < 8; ++k) v[k] = bf8(lds16(lb + L_V + k * 1024 + l16));
#pragma unroll
        for (int tt = 0; tt < 2; ++tt) {
            if (tt == 1 && w >= 2) break;
            f32x16 a2 = {};
#pragma unroll
            for (int k = 0; k < 8; ++k) a2 = mfma32(v[k], bf8(w2[tt][k]), a2);
            const int col = ch * 320 + (tt == 0 ? w : 8 + (w & 1)) * 32 + r;
            const unsigned vo = ux_slot(col >> 3, s) + (h >> 1) * 1024 + (hf * 16 + (h & 1) * 8 + (col & 7)) * 16;
            st16c(rw, pk8(a2, 0), vo, WS_UX); st16c(rw, pk8(a2, 8), vo + 512, WS_UX);
        }
        TR(3);
        if (s == 0 || s == NSEG - 1) { VMCNT(0); barrier(); if (t == 0) st4c(rw, s + 1, pr, 0); }
    }
    TRDUMP();
}

__global__ void __launch_bounds__(512, 2) k_attn(const u16* xin, const u16* Wq, const u16* Wk, const u16* Wv, const u16* Wo, char* ws, int layer, int pad) {
    extern __shared__ __attribute__((aligned(16))) char smem[];
    const unsigned lb = (unsigned)(size_t)(LAS char*)smem;
    const Ctx c = {xin, Wq, Wk, Wv, Wo, ws, layer};
    const int xcd = blockIdx.x & 7, i = blockIdx.x >> 3, j = i * 3 + xcd - 1;
    if (xcd == 0) role_head(c, i, lb);
    else if (xcd < 4 && j < 80) role_u(c, j, lb);
    else if (xcd == 5 || (xcd < 4 && j < 88)) role_x(c, xcd == 5 ? i : j - 48, lb);
    else if (xcd == 4) role_qk(c, i, lb);
    else if (xcd >= 6) role_vu(c, xcd - 6, i, lb);
}

__global__ void __launch_bounds__(512) k_ln2(char* ws) {
    __shared__ __attribute__((aligned(16))) u16 tile[32][D + 8];
    const int t = threadIdx.x, row = t >> 4, l16 = t & 15, seg = blockIdx.x;
    float v[40]; load_row40((const u16*)(ws + WS_X1), seg * 32 + row, l16, v); ln40(v); ln40(v);
#pragma unroll
    for (int i = 0; i < 5; ++i)
        *(u32x4*)&tile[row][(l16 + 16 * i) * 8] = (u32x4){pk2(v[8 * i], v[8 * i + 1]), pk2(v[8 * i + 2], v[8 * i + 3]), pk2(v[8 * i + 4], v[8 * i + 5]), pk2(v[8 * i + 6], v[8 * i + 7])};
    __syncthreads();
    for (int i = t; i < 2 * 20 * 64; i += 512) {
        const int k16 = i / 1280, dt = (i >> 6) % 20, lane = i & 63, d = dt * 32 + (lane & 31), s0 = k16 * 16 + (lane >> 5) * 8;
        unsigned o[4];
#pragma unroll
        for (int j = 0; j < 4; ++j) o[j] = tile[s0 + 2 * j][d] | ((unsigned)tile[s0 + 2 * j + 1][d] << 16);
        *(u32x4*)(ws + WS_H3 + ((size_t)(seg * 2 + k16) * 20 + dt) * 1024 + lane * 16) = (u32x4){o[0], o[1], o[2], o[3]};
    }
}
DI float gelu_t(float x) { const float z = 0.7978845608028654f * (x + 0.044715f * x * x * x); const float e = __builtin_amdgcn_exp2f(z * 2.885390081777927f); return x - x * __builtin_amdgcn_rcpf(e + 1.f); }
template <int MODE> __global__ void __launch_bounds__(256) k_mix(const u16* W, const u16* bias, char* ws, u16* out) {
    constexpr int K = MODE == 0 ? S : F, N = MODE == 0 ? F : S;
    __shared__ __attribute__((aligned(16))) char smem[2][64 * 128];
    const int t = threadIdx.x, lane = t & 63, w = rfl(t >> 6), r = lane & 31, hf = lane >> 5;
    const int n0 = (blockIdx.x / 10) * 64, dt = (blockIdx.x % 10) * 2 + (w >> 1), nt = w & 1;
    const char* X = ws + (MODE == 0 ? WS_H3 : WS_U);
    const unsigned lb = (unsigned)(size_t)(LAS char*)smem;
    const u32x4 sw = mk_srd(W, K * N * 2);
    const int q = (lane >> 2) & 3, p = lane & 3, c16 = (lane >> 4) & 1;
    const unsigned vo = (w * 16 + (lane >> 3)) * N * 2 + n0 * 2 + (lane & 7) * 16;
    auto stage = [&](int kt) { dma_p<0>(sw, vo, kt * 64 * N * 2, lb + (kt & 1) * 8192 + w * 2048); dma_p<0>(sw, vo + 8 * N * 2, kt * 64 * N * 2, lb + (kt & 1) * 8192 + w * 2048 + 1024); };
    f32x16 acc = {};
    stage(0);
    for (int kt = 0; kt < K / 64; ++kt) {
        u32x4 x[4];
#pragma unroll
        for (int k = 0; k < 4; ++k) x[k] = *(const u32x4*)(X + ((size_t)(kt * 4 + k) * 20 + dt) * 1024 + lane * 16);
        VMCNT(0); barrier();
        if (kt + 1 < K / 64) stage(kt + 1);
#pragma unroll
        for (int k = 0; k < 4; ++k) {
            const unsigned ad = lb + (kt & 1) * 8192 + (16 * k + 8 * hf + q) * 128 + (nt * 32 + 16 * c16 + 4 * p) * 2;
            acc = mfma32(bf8(ldstr(ad), ldstr(ad + 512)), bf8(x[k]), acc);
        }
    }
    if (MODE == 0) {
#pragma unroll
        for (int sI = 0; sI < 2; ++sI) {
            unsigned o[4];
#pragma unroll
            for (int j = 0; j < 4; ++j) {
                float e[2];
#pragma unroll
                for (int z = 0; z < 2; ++z) { const int reg = 8 * sI + 2 * j + z, f = n0 + nt * 32 + (reg & 3) + 8 * (reg >> 2) + 4 * hf; e[z] = gelu_t(rbf(rbf(acc[reg]) + b2f(bias[f]))); }
                o[j] = pk2(e[0], e[1]);
            }
            auto s0 = __builtin_amdgcn_permlane32_swap(o[0], o[2], false, false);
            auto s1 = __builtin_amdgcn_permlane32_swap(o[1], o[3], false, false);
            *(u32x4*)(ws + WS_U + ((size_t)((n0 + nt * 32) / 16 + sI) * 20 + dt) * 1024 + lane * 16) = (u32x4){s0[0], s1[0], s0[1], s1[1]};
        }
    } else {
        const u16* x1 = (const u16*)(ws + WS_X1);
#pragma unroll
        for (int reg = 0; reg < 16; ++reg) {
            const int s = n0 + nt * 32 + (reg & 3) + 8 * (reg >> 2) + 4 * hf, d = dt * 32 + r;
            const float y = rbf(rbf(acc[reg]) + b2f(bias[s]));
            out[(size_t)s * D + d] = (u16)(pk2(b2f(x1[(size_t)s * D + d]) + y, 0.f) & 0xffff);
        }
    }
}

extern "C" void kernel_launch(void* const* d_in, const int* in_sizes, int n_in, void* d_out, int out_size, void* d_ws, size_t ws_size, hipStream_t stream) {
    const u16 *x = (const u16*)d_in[0], *mem0 = (const u16*)d_in[1], *Wq = (const u16*)d_in[2], *Wk = (const u16*)d_in[3], *Wv = (const u16*)d_in[4], *Wo = (const u16*)d_in[5];
    const u16 *w1 = (const u16*)d_in[8], *b1 = (const u16*)d_in[9], *w2 = (const u16*)d_in[10], *b2 = (const u16*)d_in[11];
    char* ws = (char*)d_ws;
    (void)hipFuncSetAttribute((const void*)k_pre, hipFuncAttributeMaxDynamicSharedMemorySize, 81920);
    (void)hipFuncSetAttribute((const void*)k_attn, hipFuncAttributeMaxDynamicSharedMemorySize, 163840);
    k_pre<<<PRE_FOLD + PRE_LN + PRE_INIT + 1, 512, 81920, stream>>>(x, mem0, Wv, Wo, ws);
    for (int layer = 0; layer < 2; ++layer) {
        const u16* xin = layer == 0 ? x : (const u16*)(ws + WS_X2);
        if (layer == 1) k_ln1<<<128, 256, 0, stream>>>(xin, ws);
        k_attn<<<256, 512, 163840, stream>>>(xin, Wq, Wk, Wv, Wo, ws, layer, 0);
        k_ln2<<<64, 512, 0, stream>>>(ws);
        k_mix<0><<<27 * 10, 256, 0, stream>>>(w1, b1, ws, nullptr);
        k_mix<1><<<32 * 10, 256, 0, stream>>>(w2, b2, ws, layer == 0 ? (u16*)(ws + WS_X2) : (u16*)d_out);
    }
}
```

```cpp
#include <hip/hip_runtime.h>
#include <stdint.h>

#define DI __device__ __forceinline__
typedef unsigned short u16;
typedef __bf16 bf16x2 __attribute__((ext_vector_type(2)));
typedef __bf16 bf16x8 __attribute__((ext_vector_type(8)));
typedef __bf16 bf16x16 __attribute__((ext_vector_type(16)));
typedef short s16x4 __attribute__((ext_vector_type(4)));
typedef float f32x2 __attribute__((ext_vector_type(2)));
typedef float f32x4 __attribute__((ext_vector_type(4)));
typedef float f32x16 __attribute__((ext_vector_type(16)));
typedef unsigned u32x2 __attribute__((ext_vector_type(2)));
typedef unsigned u32x4 __attribute__((ext_vector_type(4)));
typedef unsigned u32x8 __attribute__((ext_vector_type(8)));
#define LAS __attribute__((address_space(3)))

constexpr int S = 2048, D = 640, H = 32, HD = 4096, F = 1728, NSEG = 64;
constexpr size_t MB = 1u << 20;
constexpr size_t WS_COMM = 0;
constexpr size_t WS_CTRL = 4 * MB;
constexpr size_t WS_PROG = WS_CTRL + 131072;
constexpr size_t WS_HF = 5 * MB;
constexpr size_t WS_X1 = 8 * MB;
constexpr size_t WS_X2 = 11 * MB;
constexpr size_t WS_H3 = 14 * MB;
constexpr size_t WS_U = 17 * MB;
constexpr size_t WS_QF = 20 * MB;
constexpr size_t WS_KF = 37 * MB;
constexpr size_t WS_BF = 54 * MB;
constexpr size_t WS_UX = 80 * MB;
constexpr size_t WS_PX = 164 * MB;
constexpr size_t WS_UM = 168 * MB;
constexpr size_t WS_END = 190 * MB;
#define PRING 8
constexpr unsigned C_MEM = 0, C_P = 655360, C_FH = C_P + PRING * 32768, C_END = C_FH + 4096;
constexpr int CT_ABORT = 224, CT_NEAR = 240, CT_STAT = 256;
constexpr int PG_R = 0, PG_U = 32, PG_QK = 112, PG_VU = 144, PG_X = 208, PG_L = 248, PG_G1 = 256, PG_G2 = 352, PG_HF = 608;
constexpr unsigned RETRY_CAP = 1u << 14;
#define CU 100
#define CH 84
#define G2SLEEP 4
#define G1SLEEP 4
#define LSLEEP 4
#define AGG1 60
#define AGG2 4
#define BKH 0
#define BKU 0
#define XSLEEP 6
#define PF_SLEEP 16

DI float b2f(u16 x) { return __uint_as_float((unsigned)x << 16); }
DI unsigned pk2(float lo, float hi) { f32x2 v = {lo, hi}; return __builtin_bit_cast(unsigned, __builtin_convertvector(v, bf16x2)); }
DI float rbf(float x) { return __uint_as_float(pk2(x, x) << 16); }
DI float blo(unsigned w) { return __uint_as_float(w << 16); }
DI float bhi(unsigned w) { return __uint_as_float(w & 0xffff0000u); }
DI unsigned cmap(unsigned off, unsigned nearbit) { return ((off >> 13) << 14) + (nearbit << 13) + (off & 8191u); }
DI u32x4 mk_srd(const void* p, unsigned bytes) { u32x4 r; r.x = (unsigned)(size_t)p; r.y = (unsigned)((size_t)p >> 32); r.z = bytes; r.w = 0x00020000u; return r; }
DI unsigned rfl(unsigned x) { return __builtin_amdgcn_readfirstlane(x); }
DI unsigned clk() { unsigned long long t; asm volatile("s_memtime %0\n\ts_waitcnt lgkmcnt(0)" : "=s"(t)::"memory"); return (unsigned)t; }
DI unsigned rtc() { unsigned long long t; asm volatile("s_memrealtime %0\n\ts_waitcnt lgkmcnt(0)" : "=s"(t)::"memory"); return (unsigned)t; }
DI void barrier() { asm volatile("s_waitcnt lgkmcnt(0)\n\ts_barrier" ::: "memory"); }
#define SB() __builtin_amdgcn_sched_barrier(0)
#define VMCNT(n) asm volatile("s_waitcnt vmcnt(" #n ")" ::: "memory")
template <int OFF> DI void dma_c(u32x4 srd, unsigned voff, unsigned soff, unsigned lds) {
    asm volatile("s_mov_b32 m0, %0\n\ts_nop 0\n\tbuffer_load_dwordx4 %1, %2, %3 offen offset:%4 sc1 lds" ::"s"(lds), "v"(voff), "s"(srd), "s"(soff), "i"(OFF) : "memory");
}
template <int OFF> DI void dma_p(u32x4 srd, unsigned voff, unsigned soff, unsigned lds) {
    asm volatile("s_mov_b32 m0, %0\n\ts_nop 0\n\tbuffer_load_dwordx4 %1, %2, %3 offen offset:%4 lds" ::"s"(lds), "v"(voff), "s"(srd), "s"(soff), "i"(OFF) : "memory");
}
DI __amdgpu_buffer_rsrc_t mk_rs(const void* p, unsigned bytes) { return __builtin_amdgcn_make_buffer_rsrc((void*)p, 0, bytes, 0x00020000); }
DI void st16c(__amdgpu_buffer_rsrc_t r, u32x4 v, unsigned voff, unsigned soff) { __builtin_amdgcn_raw_buffer_store_b128(v, r, voff, soff, 16); }
DI void st8c(__amdgpu_buffer_rsrc_t r, u32x2 v, unsigned voff, unsigned soff) { __builtin_amdgcn_raw_buffer_store_b64(v, r, voff, soff, 16); }
DI void st4c(__amdgpu_buffer_rsrc_t r, unsigned v, unsigned voff, unsigned soff) { __builtin_amdgcn_raw_buffer_store_b32(v, r, voff, soff, 16); }
DI u32x4 ld16c(__amdgpu_buffer_rsrc_t r, unsigned voff, unsigned soff) { return __builtin_amdgcn_raw_buffer_load_b128(r, voff, soff, 16); }
DI u32x2 ld8c(__amdgpu_buffer_rsrc_t r, unsigned voff, unsigned soff) { return __builtin_amdgcn_raw_buffer_load_b64(r, voff, soff, 16); }
DI unsigned ld4c(__amdgpu_buffer_rsrc_t r, unsigned voff, unsigned soff) { return __builtin_amdgcn_raw_buffer_load_b32(r, voff, soff, 16); }
DI u32x4 lds16(unsigned a) { return *(LAS u32x4*)a; }
DI u32x2 lds8(unsigned a) { return *(LAS u32x2*)a; }
DI void lds16w(unsigned a, u32x4 v) { *(LAS u32x4*)a = v; }
DI void lds4w(unsigned a, unsigned v) { *(LAS unsigned*)a = v; }
DI u32x2 ldstr(unsigned a) { return __builtin_bit_cast(u32x2, __builtin_amdgcn_ds_read_tr16_b64_v4i16((LAS s16x4*)a)); }
DI bf16x8 bf8(u32x4 v) { return __builtin_bit_cast(bf16x8, v); }
DI bf16x8 bf8(u32x2 a, u32x2 b) { u32x4 v = {a.x, a.y, b.x, b.y}; return __builtin_bit_cast(bf16x8, v); }
DI f32x4 mfma16(bf16x8 a, bf16x8 b, f32x4 c) { return __builtin_amdgcn_mfma_f32_16x16x32_bf16(a, b, c, 0, 0, 0); }
DI f32x4 mfma16k16(u32x2 a, u32x2 b, f32x4 c) { return mfma16(bf8(a, (u32x2){0, 0}), bf8(b, (u32x2){0, 0}), c); }
DI f32x16 mfma32(bf16x8 a, bf16x8 b, f32x16 c) { return __builtin_amdgcn_mfma_f32_32x32x16_bf16(a, b, c, 0, 0, 0); }
DI f32x4 smfma(u32x4 a, u32x8 b, f32x4 c, int idx) { return __builtin_amdgcn_smfmac_f32_16x16x64_bf16(bf8(a), __builtin_bit_cast(bf16x16, b), c, idx, 0, 0); }
DI float halfsum(float a, float b) { auto r = __builtin_amdgcn_permlane32_swap(__float_as_uint(a), __float_as_uint(b), false, false); return __uint_as_float(r[0]) + __uint_as_float(r[1]); }
DI float xg_max(float x) {
    auto s = __builtin_amdgcn_permlane16_swap(__float_as_uint(x), __float_as_uint(x), false, false); x = fmaxf(__uint_as_float(s[0]), __uint_as_float(s[1]));
    auto t = __builtin_amdgcn_permlane32_swap(__float_as_uint(x), __float_as_uint(x), false, false); return fmaxf(__uint_as_float(t[0]), __uint_as_float(t[1]));
}
DI float xg_sum(float x) {
    auto s = __builtin_amdgcn_permlane16_swap(__float_as_uint(x), __float_as_uint(x), false, false); x = __uint_as_float(s[0]) + __uint_as_float(s[1]);
    auto t = __builtin_amdgcn_permlane32_swap(__float_as_uint(x), __float_as_uint(x), false, false); return __uint_as_float(t[0]) + __uint_as_float(t[1]);
}
DI unsigned max3u(unsigned a, unsigned b, unsigned c) { return max(max(a, b), c); }

DI int sp_row(int gB, int a, int pos) { return 16 * (2 * (a >> 1) + (gB >> 1)) + 8 * (pos >> 1) + 2 * (2 * (gB & 1) + (a & 1)) + (pos & 1); }

template <int CTRL> DI float dppf(float x) { return __builtin_bit_cast(float, __builtin_amdgcn_mov_dpp(__builtin_bit_cast(int, x), CTRL, 0xf, 0xf, true)); }
DI float row16_sum(float x) { x += dppf<0xB1>(x); x += dppf<0x4E>(x); x += dppf<0x141>(x); x += dppf<0x128>(x); return x; }
DI void ln40(float (&v)[40]) {
    float s = 0;
#pragma unroll
    for (int i = 0; i < 40; ++i) s += v[i];
    const float mean = row16_sum(s) * (1.f / D);
    float q = 0;
#pragma unroll
    for (int i = 0; i < 40; ++i) { float d = v[i] - mean; q += d * d; }
    const float var = rbf(row16_sum(q) * (1.f / D)), m = rbf(mean), inv = 1.f / rbf(sqrtf(rbf(var + 1e-5f)));
#pragma unroll
    for (int i = 0; i < 40; i += 2) { const unsigned a = pk2(v[i] - m, v[i + 1] - m), b = pk2(blo(a) * inv, bhi(a) * inv); v[i] = blo(b); v[i + 1] = bhi(b); }
}
DI void unpack8(u32x4 w, float* v) {
#pragma unroll
    for (int j = 0; j < 4; ++j) { v[2 * j] = blo(w[j]); v[2 * j + 1] = bhi(w[j]); }
}
DI u32x4 pack8(const float* v) { return (u32x4){pk2(v[0], v[1]), pk2(v[2], v[3]), pk2(v[4], v[5]), pk2(v[6], v[7])}; }
DI unsigned hf_off(int row, int p) { return (row >> 5) * 40960 + (p >> 1) * 1024 + ((p & 1) * 32 + (row & 31)) * 16; }
constexpr int PRE_FOLD = 320, PRE_LN = 64, PRE_INIT = 64;
__global__ void __launch_bounds__(512) k_pre(const u16* x, const u16* mem0, const u16* Wv, const u16* Wo, char* ws) {
    extern __shared__ __attribute__((aligned(16))) char smem[];
    const int b = blockIdx.x, t = threadIdx.x, lane = t & 63, w = rfl(t >> 6);
    if (b < PRE_FOLD) {
        const int h = b / 10, ksp = (b % 10) >> 1, ch = b & 1;
        const unsigned lb = (unsigned)(size_t)(LAS char*)smem;
        const u32x4 so = mk_srd(Wo, HD * D * 2);
#pragma unroll
        for (int i = 0; i < 10; ++i) {
            const unsigned by = (w * 10 + i) * 1024 + lane * 16;
            dma_p<0>(so, (h * 128 + by / 640) * 1280 + ch * 640 + by % 640, 0, lb + (w * 10 + i) * 1024);
        }
        const int rt = w & 3, cg = w >> 2, r = lane & 31, hf = lane >> 5;
        const int ks = ksp * 2 + (rt >> 1), T = rt & 1;
        const int d = 64 * ks + sp_row(2 * T + ((r >> 2) & 1), r >> 3, r & 3);
        u32x4 a[8];
#pragma unroll
        for (int k = 0; k < 8; ++k) a[k] = *(const u32x4*)(Wv + (size_t)d * HD + h * 128 + 16 * k + 8 * hf);
        VMCNT(0); barrier();
        const int q = (lane >> 2) & 3, p = lane & 3, c16 = (lane >> 4) & 1;
#pragma unroll
        for (int ct = 0; ct < 5; ++ct) {
            const int col0 = (cg * 5 + ct) * 32;
            f32x16 acc = {};
#pragma unroll
            for (int k = 0; k < 8; ++k) {
                const unsigned ad = lb + (16 * k + 8 * hf + q) * 640 + (col0 + 16 * c16 + 4 * p) * 2;
                acc = mfma32(bf8(a[k]), bf8(ldstr(ad), ldstr(ad + 4 * 640)), acc);
            }
            const int col = ch * 320 + col0 + r, u = col >> 3, gB = 2 * T + hf;
            char* dst = ws + WS_BF + ((size_t)((u * 8 + (h >> 2)) * 2 + ((h >> 1) & 1)) * 10 + ks) * 2048 + (gB * 16 + (h & 1) * 8 + (col & 7)) * 16;
            *(u32x4*)dst = (u32x4){pk2(acc[0], acc[1]), pk2(acc[2], acc[3]), pk2(acc[4], acc[5]), pk2(acc[6], acc[7])};
            *(u32x4*)(dst + 1024) = (u32x4){pk2(acc[8], acc[9]), pk2(acc[10], acc[11]), pk2(acc[12], acc[13]), pk2(acc[14], acc[15])};
        }
    } else if (b < PRE_FOLD + PRE_LN) {
        const int row = (b - PRE_FOLD) * 32 + (t >> 4), l16 = t & 15;
        float v[40];
#pragma unroll
        for (int i = 0; i < 5; ++i) unpack8(*(const u32x4*)(x + (size_t)row * D + (l16 + 16 * i) * 8), v + 8 * i);
        ln40(v);
#pragma unroll
        for (int i = 0; i < 5; ++i) *(u32x4*)(ws + WS_HF + hf_off(row, l16 + 16 * i)) = pack8(v + 8 * i);
    } else if (b < PRE_FOLD + PRE_LN + PRE_INIT) {
        const int ib = b - PRE_FOLD - PRE_LN;
        for (unsigned i = ib * 512 + t; i < 2 * 2 * 65536; i += PRE_INIT * 512) {
            const unsigned layer = i >> 17, half = (i >> 16) & 1, off = (i & 65535) * 16;
            u32x4 v;
            if (off < 10240) {
                const unsigned u = off >> 7, m = (off >> 4) & 7;
                v = *(const u32x4*)(mem0 + m * D + u * 8);
                v.x = min(v.x, 0xFFFFFFFEu); v.y = min(v.y, 0xFFFFFFFEu); v.z = min(v.z, 0xFFFFFFFEu); v.w = min(v.w, 0xFFFFFFFEu);
            } else if (off < C_P) v = (u32x4){~0u, ~0u, ~0u, ~0u};
            else v = (u32x4){0, 0, 0, 0};
            if (off < C_END) *(u32x4*)(ws + WS_COMM + layer * 2 * MB + cmap(off, half)) = v;
        }
        if (ib == 0) { for (int i = t; i < 2048; i += 512) if ((i & 1023) != CT_NEAR) ((unsigned*)(ws + WS_CTRL))[i] = 0; }
        if (t < 32) ((unsigned*)(ws + WS_PROG))[(ib * 32 + t) * 32] = 0;
    } else if (t == 0) {
        const unsigned xcc = (unsigned)__builtin_amdgcn_s_getreg(20 | (3 << 11)) & 0xfu;
        for (int layer = 0; layer < 2; ++layer) {
            unsigned best[2] = {~0u, ~0u};
            for (int r = 0; r < 6; ++r) {
                char* p = ws + WS_COMM + layer * 2 * MB + 2 * MB - 16384 + (r & 1) * 8192;
                unsigned v;
                asm volatile("global_store_dword %0, %1, off sc1\n\ts_waitcnt vmcnt(0)" ::"v"(p), "v"(r) : "memory");
                const unsigned t0 = clk();
                asm volatile("global_load_dword %0, %1, off sc1\n\ts_waitcnt vmcnt(0)" : "=&v"(v) : "v"(p) : "memory");
                best[r & 1] = min(best[r & 1], clk() - t0);
            }
            ((unsigned*)(ws + WS_CTRL))[layer * 1024 + CT_NEAR] = (best[0] < best[1] ? 0u : 1u) ^ (xcc >= 4 ? 1u : 0u);
        }
    }
}

constexpr float SC_LOG2E = 0.08838834764831845f * 1.4426950408889634f, NEG = -1e30f;
DI float mx3(float a, float b, float c) { return __builtin_fmaxf(__builtin_fmaxf(a, b), c); }
DI f32x2 lo2(f32x4 v) { return (f32x2){v[0], v[1]}; }
DI f32x2 hi2(f32x4 v) { return (f32x2){v[2], v[3]}; }
DI f32x2 ex2(f32x2 v) { return (f32x2){__builtin_amdgcn_exp2f(v[0]), __builtin_amdgcn_exp2f(v[1])}; }
DI unsigned pk2v(f32x2 v) { return __builtin_bit_cast(unsigned, __builtin_convertvector(v, bf16x2)); }
DI void softmax40(f32x4 sx0, f32x4 sx1, f32x4 sm, u32x4& px, u32x2& pm) {
    float mx = mx3(mx3(mx3(sx0[0], sx0[1], sx0[2]), sx0[3], sx1[0]), mx3(sx1[1], sx1[2], sx1[3]), mx3(mx3(sm[0], sm[1], sm[2]), sm[3], NEG));
    { auto r = __builtin_amdgcn_permlane16_swap(__float_as_uint(mx), __float_as_uint(mx), false, false); mx = mx3(__uint_as_float(r[0]), __uint_as_float(r[1]), NEG); }
    { auto r = __builtin_amdgcn_permlane32_swap(__float_as_uint(mx), __float_as_uint(mx), false, false); mx = mx3(__uint_as_float(r[0]), __uint_as_float(r[1]), NEG); }
    mx *= SC_LOG2E;
    const f32x2 c = {SC_LOG2E, SC_LOG2E}, m = {mx, mx};
    const f32x2 e0 = ex2(lo2(sx0) * c - m), e1 = ex2(hi2(sx0) * c - m), e2 = ex2(lo2(sx1) * c - m), e3 = ex2(hi2(sx1) * c - m), e4 = ex2(lo2(sm) * c - m), e5 = ex2(hi2(sm) * c - m);
    const f32x2 t = ((e0 + e1) + (e2 + e3)) + (e4 + e5);
    const float inv = __builtin_amdgcn_rcpf(xg_sum(t[0] + t[1]));
    const f32x2 iv = {inv, inv};
    px = (u32x4){pk2v(e0 * iv), pk2v(e1 * iv), pk2v(e2 * iv), pk2v(e3 * iv)};
    pm = (u32x2){pk2v(e4 * iv), pk2v(e5 * iv)};
}

DI f32x2 halfsum2(f32x4 d) {
    auto a = __builtin_amdgcn_permlane32_swap(__float_as_uint(d[0]), __float_as_uint(d[2]), false, false);
    auto b = __builtin_amdgcn_permlane32_swap(__float_as_uint(d[1]), __float_as_uint(d[3]), false, false);
    return (f32x2){__uint_as_float(a[0]), __uint_as_float(b[0])} + (f32x2){__uint_as_float(a[1]), __uint_as_float(b[1])};
}
#define ROW16_STEP(op, c) op "_dpp %0, %0, %0 " c " row_mask:0xf bank_mask:0xf\n\t" op "_dpp %1, %1, %1 " c " row_mask:0xf bank_mask:0xf\n\ts_nop 0\n\t"
#define ROW16(op, a, b) asm("s_nop 1\n\t" ROW16_STEP(op, "quad_perm:[1,0,3,2]") ROW16_STEP(op, "quad_perm:[2,3,0,1]") ROW16_STEP(op, "row_half_mirror") ROW16_STEP(op, "row_mirror") : "+v"(a), "+v"(b))

#define MARK(i)
struct Ctx { const u16 *x, *Wq, *Wk, *Wv, *Wo, *w1, *b1, *w2, *b2; u16* out; char* ws; };
DI Ctx args() { const __attribute__((address_space(4))) Ctx* p = (const __attribute__((address_space(4))) Ctx*)__builtin_amdgcn_kernarg_segment_ptr(); asm volatile("" : "+s"(p));
    return Ctx{p->x, p->Wq, p->Wk, p->Wv, p->Wo, p->w1, p->b1, p->w2, p->b2, p->out, p->ws}; }
DI unsigned ux_slot(int u, int s) { return (u * 64 + ((s + 5 * u) & 63)) * 16384; }
DI unsigned qk_slot(int h, int s) { return (h * 64 + ((s + 3 * h) & 63)) * 8192; }
DI unsigned pg(int layer, int line) { return WS_PROG + layer * 131072 + line * 128; }
DI unsigned sopaque(unsigned x) { x = rfl(x); asm volatile("" : "+s"(x)); return x; }
DI unsigned vopaque(unsigned x) { asm volatile("" : "+v"(x)); return x; }

DI unsigned wave_min(unsigned v) {
    v = min(v, (unsigned)__builtin_amdgcn_mov_dpp(v, 0xB1, 0xf, 0xf, true)); v = min(v, (unsigned)__builtin_amdgcn_mov_dpp(v, 0x4E, 0xf, 0xf, true));
    v = min(v, (unsigned)__builtin_amdgcn_mov_dpp(v, 0x141, 0xf, 0xf, true)); v = min(v, (unsigned)__builtin_amdgcn_mov_dpp(v, 0x140, 0xf, 0xf, true));
    return min(min((unsigned)__builtin_amdgcn_readlane(v, 0), (unsigned)__builtin_amdgcn_readlane(v, 16)), min((unsigned)__builtin_amdgcn_readlane(v, 32), (unsigned)__builtin_amdgcn_readlane(v, 48)));
}
template <int SLEEP> DI void wait_prog(__amdgpu_buffer_rsrc_t rc, unsigned voff, unsigned need, unsigned& have, unsigned mul = 1, unsigned add = 0) {
    for (unsigned n = 0; have < need; ++n) {
        asm volatile("" ::: "memory");
        have = wave_min(ld4c(rc, voff, 0) * mul + add);
        if (n > RETRY_CAP * 4) { st4c(rc, 1, WS_CTRL + CT_ABORT * 4, 0); have = 1000; }
        if (have < need) __builtin_amdgcn_s_sleep(SLEEP);
    }
}
DI unsigned newest_stamp(__amdgpu_buffer_rsrc_t rc, unsigned voff, unsigned want, unsigned& alive, unsigned lds) {
    unsigned now = rtc();
    u32x2 v = ld8c(rc, voff, 0);
    for (unsigned n = 0; alive && (unsigned)__builtin_amdgcn_ballot_w64(v.x < want) != 0; ++n) {
        if (n > RETRY_CAP * 4) alive = 0;
        asm volatile("" ::: "memory");
        v = ld8c(rc, voff, 0);
    }
    int d = (int)(v.y - now);
    d = max(d, __builtin_amdgcn_mov_dpp(d, 0xB1, 0xf, 0xf, true)); d = max(d, __builtin_amdgcn_mov_dpp(d, 0x4E, 0xf, 0xf, true));
    d = max(d, __builtin_amdgcn_mov_dpp(d, 0x141, 0xf, 0xf, true)); d = max(d, __builtin_amdgcn_mov_dpp(d, 0x140, 0xf, 0xf, true));
    if (alive) now += max(__builtin_amdgcn_readlane(d, 0), __builtin_amdgcn_readlane(d, 16));
    *(volatile LAS u32x2*)lds = (u32x2){want, now};
    return now;
}
DI unsigned lds_stamp(unsigned want, unsigned lds) {
    for (;;) { const u32x2 v = *(volatile LAS u32x2*)lds; if (rfl(v.x) == want) return rfl(v.y); }
}
DI void wait_rt(unsigned target) { while ((int)(rtc() - target) < 0) {} }
DI float gelu_t(float x) { const float z = 0.7978845608028654f * (x + 0.044715f * x * x * x); const float e = __builtin_amdgcn_exp2f(z * 2.885390081777927f); return x - x * __builtin_amdgcn_rcpf(e + 1.f); }

DI void phase_mix2(int layer, unsigned lb) {
    const Ctx c = args();
    constexpr int NST = 14, STG = 36864;
    const int t = vopaque(threadIdx.x), lane = t & 63, w = rfl(t >> 6), g = lane >> 4, col = lane & 15, nt = w & 3, kh = w >> 2;
    const int db = (blockIdx.x & 1) * 4 + ((blockIdx.x >> 3) & 3), nb = (blockIdx.x >> 5) * 4 + ((blockIdx.x >> 1) & 3);
    const u32x4 sw = mk_srd(c.w2, F * S * 2), sx = mk_srd(c.ws + WS_U, F * D * 2);
    const __amdgpu_buffer_rsrc_t rw = mk_rs(c.ws, WS_END);
    const unsigned wr = lane >> 3, vw = wr * S * 2 + nb * 128 + (((lane & 7) ^ (((wr >> 1) & 3) << 1)) * 16), l16 = lane * 16;
    auto stage_w = [&](int st) {
        const unsigned l0 = lb + (st & 3) * STG + w * 2048;
        dma_p<0>(sw, vw, (st * 128 + w * 16) * S * 2, l0); dma_p<0>(sw, vw, (st * 128 + w * 16 + 8) * S * 2, l0 + 1024);
    };
    auto stage_x = [&](int st) {
        const unsigned xs = (st * 4 + nt) * 40960 + db * 5120, xl = lb + (st & 3) * STG + 16384 + nt * 5120;
        if (kh == 0) { dma_c<0>(sx, l16, xs, xl); dma_c<1024>(sx, l16, xs, xl); dma_c<2048>(sx, l16, xs, xl); }
        else { dma_c<3072>(sx, l16, xs, xl); dma_c<0>(sx, l16, xs + 4096, xl + 4096); }
    };
    barrier();
    stage_w(0); stage_w(1); stage_w(2); stage_w(3);
    const int n4 = nb * 64 + nt * 16;
    const float bs = b2f(c.b2[n4 + col]);
    if (w == 7) { unsigned have = 0; wait_prog<G2SLEEP>(rw, pg(layer, PG_G1 + db * 12 + lane % 12), 1, have); }
    barrier();
    MARK(8);
    const unsigned oo = ((n4 + col) * D + db * 80 + kh * 48 + 4 * g) * 2;
    u32x2 xr[3];
#pragma unroll
    for (int i = 0; i < 3; ++i) xr[i] = ld8c(rw, oo + (kh == 1 && i == 2 ? 0 : i * 32), WS_X1);
    stage_x(0); stage_x(1); stage_x(2); stage_x(3);
    f32x4 acc[5] = {};
    const unsigned ar = 4 * g + ((lane >> 2) & 3), ao = kh * 8192 + ar * 128 + (((nt ^ ((ar >> 1) & 3)) * 32) + (lane & 3) * 8), bo = 16384 + kh * 10240 + l16;
    u32x2 a0[2], a1[2]; u32x4 b0[5], b1[5];
    auto rd = [&](int st, int k, u32x2 (&a)[2], u32x4 (&b)[5]) {
        const unsigned l0 = lb + (st & 3) * STG;
        a[0] = ldstr(l0 + ao + k * 4096); a[1] = ldstr(l0 + ao + k * 4096 + 2048);
#pragma unroll
        for (int i = 0; i < 5; ++i) b[i] = lds16(l0 + bo + (k * 5 + i) * 1024);
    };
    if (kh == 0) VMCNT(9); else VMCNT(6);
    barrier();
    MARK(11);
    rd(0, 0, a0, b0);
    for (int st = 0; st < NST; ++st) {
        rd(st, 1, a1, b1);
        SB();
#pragma unroll
        for (int i = 0; i < 5; ++i) acc[i] = mfma16(bf8(b0[i]), bf8(a0[0], a0[1]), acc[i]);
        SB();
        if (kh == 0) { if (st == 0) VMCNT(6); else if (st == 1) VMCNT(8); else if (st + 3 < NST) VMCNT(10); else if (st + 2 < NST) VMCNT(5); else VMCNT(0); }
        else { if (st == 0) VMCNT(4); else if (st == 1) VMCNT(6); else if (st + 3 < NST) VMCNT(8); else if (st + 2 < NST) VMCNT(4); else VMCNT(0); }
        barrier();
        if (st + 1 < NST) rd(st + 1, 0, a0, b0);
        SB();
#pragma unroll
        for (int i = 0; i < 5; ++i) acc[i] = mfma16(bf8(b1[i]), bf8(a1[0], a1[1]), acc[i]);
        SB();
        if (st + 4 < NST) { stage_w(st + 4); stage_x(st + 4); }
    }
    barrier();
    MARK(12);
    const unsigned ex = lb + nt * 5120 + l16;
    if (kh == 0) { lds16w(ex + 3072, __builtin_bit_cast(u32x4, acc[3])); lds16w(ex + 4096, __builtin_bit_cast(u32x4, acc[4])); }
    else { lds16w(ex, __builtin_bit_cast(u32x4, acc[0])); lds16w(ex + 1024, __builtin_bit_cast(u32x4, acc[1])); lds16w(ex + 2048, __builtin_bit_cast(u32x4, acc[2])); acc[0] = acc[3]; acc[1] = acc[4]; }
    barrier();
#pragma unroll
    for (int i = 0; i < 3; ++i) {
        if (kh == 1 && i == 2) break;
        const f32x4 v = acc[i] + __builtin_bit_cast(f32x4, lds16(ex + (kh * 3 + i) * 1024));
        const u32x2 o = {pk2(blo(xr[i].x) + rbf(rbf(v[0]) + bs), bhi(xr[i].x) + rbf(rbf(v[1]) + bs)), pk2(blo(xr[i].y) + rbf(rbf(v[2]) + bs), bhi(xr[i].y) + rbf(rbf(v[3]) + bs))};
        if (layer == 1) *(u32x2*)((char*)c.out + oo + i * 32) = o;
        else st8c(rw, o, oo + i * 32, WS_X2);
    }
    MARK(9);
    if (layer == 1) return;
    VMCNT(0); barrier();
    if (t == 0) st4c(rw, 1, pg(layer, PG_G2 + nb * 8 + db), 0);
    if (w < 2) {
        unsigned have = 0;
        wait_prog<1>(rw, pg(layer, PG_G2 + nb * 8 + (lane & 7)), 1, have);
        const int row = nb * 64 + db * 8 + (t >> 4), i16 = t & 15;
        float v[40];
#pragma unroll
        for (int i = 0; i < 5; ++i) unpack8(ld16c(rw, row * D * 2 + (i16 + 16 * i) * 16, WS_X2), v + 8 * i);
        ln40(v);
#pragma unroll
        for (int i = 0; i < 5; ++i) st16c(rw, pack8(v + 8 * i), hf_off(row, i16 + 16 * i), WS_HF);
        VMCNT(0);
    }
    barrier();
    if (t == 0) st4c(rw, 1, pg(layer, PG_HF + nb * 8 + db), 0);
    MARK(10);
}

DI void phase_mix1(int layer, int fb, int db, unsigned lb) {
    const Ctx c = args();
    constexpr unsigned STG = 14336, L_N = 8 * STG;
    const int t = vopaque(threadIdx.x), lane = t & 63, w = rfl(t >> 6), g = lane >> 4;
    const u32x4 sw = mk_srd(c.w1, S * F * 2), sx = mk_srd(c.ws, WS_END);
    const __amdgpu_buffer_rsrc_t rw = mk_rs(c.ws, WS_END);
    const unsigned l16 = lane * 16, b0 = w * 1024 + l16, b8 = 8192 + l16;
    const unsigned vw = (b0 / 288) * F * 2 + fb * 288 + b0 % 288, vw8 = (b8 / 288) * F * 2 + fb * 288 + b8 % 288;
    const unsigned ao = (4 * g + ((lane >> 2) & 3)) * 288 + (lane & 3) * 8;
    f32x4 acc[6] = {};
    unsigned have = 0;
    const u32x2 bw0 = *(const u32x2*)(c.b1 + fb * 144 + w * 16 + 4 * g), bw8 = *(const u32x2*)(c.b1 + fb * 144 + 128 + 4 * g);
    bool warm = false;
    for (int s0 = 0; s0 < NSEG;) {
        if (w == 7) { wait_prog<G1SLEEP>(rw, pg(layer, PG_L + lane % 7), s0 + 1, have, 7, lane % 7); lds4w(lb + L_N, have); }
        barrier();
        const int n = min(min((int)rfl(*(LAS unsigned*)(lb + L_N)), NSEG) - s0, 8);
        for (int i = 0; i < n; ++i) {
            const unsigned l0 = lb + i * STG;
            dma_p<0>(sw, vw, (s0 + i) * 32 * F * 2, l0 + w * 1024);
            if (w == 0) dma_p<0>(sw, vw8, (s0 + i) * 32 * F * 2, l0 + 8192);
            else if (w < 6) dma_c<0>(sx, l16, WS_H3 + (s0 + i) * 40960 + (db * 5 + w - 1) * 1024, l0 + 9216 + (w - 1) * 1024);
        }
        VMCNT(0); barrier();
        for (int i = 0; i < n; ++i) {
            const unsigned l0 = lb + i * STG;
            const bf16x8 a = bf8(ldstr(l0 + ao + w * 32), ldstr(l0 + ao + w * 32 + 16 * 288)), a8 = bf8(ldstr(l0 + ao + 256), ldstr(l0 + ao + 256 + 16 * 288));
#pragma unroll
            for (int k = 0; k < 5; ++k) acc[k] = mfma16(a, bf8(lds16(l0 + 9216 + k * 1024 + l16)), acc[k]);
            acc[5] = mfma16(a8, bf8(lds16(l0 + 9216 + min(w, 4) * 1024 + l16)), acc[5]);
        }
        s0 += n;
        if (!warm && s0 >= 48) {
            warm = true;
            barrier();
            const u32x4 s2 = mk_srd(c.w2, F * S * 2);
#pragma unroll
            for (int i = 0; i < 9; ++i) dma_p<0>(s2, l16, (((db * 12 + fb) * 8 + w) * 9 + i) * 1024, lb + w * 1024);
            VMCNT(0);
        }
    }
    MARK(6);
#pragma unroll
    for (int k = 0; k < 6; ++k) {
        if (k == 5 && w >= 5) break;
        const int n4 = fb * 144 + (k == 5 ? 8 : w) * 16, dt = db * 5 + (k == 5 ? w : k);
        const u32x2 bw = k == 5 ? bw8 : bw0;
        const f32x4 v = acc[k];
        const u32x2 o = {pk2(gelu_t(rbf(rbf(v[0]) + blo(bw.x))), gelu_t(rbf(rbf(v[1]) + bhi(bw.x)))), pk2(gelu_t(rbf(rbf(v[2]) + blo(bw.y))), gelu_t(rbf(rbf(v[3]) + bhi(bw.y))))};
        st8c(rw, o, l16 + ((n4 >> 4) & 1) * 8, WS_U + (n4 >> 5) * 40960 + dt * 1024);
    }
    VMCNT(0); barrier();
    if (t == 0) st4c(rw, 1, pg(layer, PG_G1 + db * 12 + fb), 0);
    MARK(7);
}

DI void role_head(int h, unsigned lb) {
    const Ctx c = args();
    const int t = threadIdx.x, lane = t & 63, w = rfl(t >> 6), g = lane >> 4, col = lane & 15;
    constexpr unsigned L_MEM = 0, L_KQ = 10240  , L_QI = L_KQ + 32768, L_KI = L_QI + 2048, L_T = L_KI + 2048;
    const u32x4 sw = mk_srd(c.ws, WS_END);
    const __amdgpu_buffer_rsrc_t rw = mk_rs(c.ws, WS_END);
    u32x8 wq[10], wk[10];
    {
        const u32x4 sq = mk_srd(c.Wq, D * HD * 2), sk = mk_srd(c.Wk, D * HD * 2);
        const unsigned vo = (w * 4 + (lane >> 4)) * 8192 + h * 256 + (lane & 15) * 16;
        auto stage = [&](int st) {
            const unsigned l0 = lb + (st & 1) * 65536;
#pragma unroll
            for (int i = 0; i < 4; ++i) { dma_p<0>(sq, vo, (st * 128 + i * 32) * 8192, l0 + i * 8192 + w * 1024); dma_p<0>(sk, vo, (st * 128 + i * 32) * 8192, l0 + 32768 + i * 8192 + w * 1024); }
        };
        stage(0);
#pragma unroll
        for (int st = 0; st < 5; ++st) {
            if (st < 4) { stage(st + 1); VMCNT(8); } else VMCNT(0);
            barrier();
#pragma unroll
            for (int k2 = 0; k2 < 2; ++k2)
#pragma unroll
                for (int a = 0; a < 4; ++a) {
                    const unsigned ad = lb + (st & 1) * 65536 + (k2 * 64 + sp_row(g, a, (lane >> 2) & 3)) * 256 + w * 32 + (lane & 3) * 8;
                    const u32x2 x = ldstr(ad), y = ldstr(ad + 32768);
                    wq[st * 2 + k2][2 * a] = x.x; wq[st * 2 + k2][2 * a + 1] = x.y; wk[st * 2 + k2][2 * a] = y.x; wk[st * 2 + k2][2 * a + 1] = y.y;
                }
            barrier();
        }
    }
    for (int layer = 0; layer < 2; ++layer) {
        MARK(0);
        const int lv = vopaque(lane), gv = lv >> 4, cv = lv & 15;
        const unsigned comm = WS_COMM + layer * 2 * MB, l16 = lv * 16;
        const unsigned nearbit = sopaque(*(const unsigned*)(c.ws + WS_CTRL + layer * 4096 + CT_NEAR * 4));
        const int idx = (lv & 8) ? 0xEEEE : 0x4444;
        unsigned have = 0, nretry = 0, alive = ~0u;
        auto prefetch = [&](int s) {
            const unsigned src = ((w & 2) ? WS_QF : WS_KF) + qk_slot(h, s) + (w & 1) * 4096, dst = lb + L_KQ + (s & 1) * 16384 + (w - 4) * 4096;
            dma_c<0>(sw, l16, src, dst); dma_c<1024>(sw, l16, src, dst); dma_c<2048>(sw, l16, src, dst); dma_c<3072>(sw, l16, src, dst);
        };
        if (w >= 4) { wait_prog<2>(rw, pg(layer, PG_QK + h), 1, have); prefetch(0); }
        const unsigned tra = (16 * (gv >> 1) + 4 * (gv & 1) + ((lv >> 2) & 3)) * 16 + (lv & 1) * 8;
        const unsigned trq = (32 * (gv >> 1) + 16 * (gv & 1) + 8 * ((lv >> 3) & 1) + ((lv >> 2) & 1) + (lv & 2)) * 16 + (lv & 1) * 8;
        const unsigned ia = (w * 16 + cv) * 16 + (4 * (gv & 1) + 2 * (gv >> 1)) * 2;
        const unsigned fh = comm + cmap(C_FH, nearbit);
        const unsigned po = (h & 3) * 640 + cv * 16 + (2 * (gv & 1) + (gv >> 1)) * 4, pm_o = cv < 8 ? po + 512 : 0x80000000u;
        const float mneg = cv < 8 ? 0.f : NEG;
        const unsigned vmask = vopaque(0x7fff7fffu);
        if (t == 0) lds4w(lb + L_T, 0);
        for (int s = 0; s < NSEG; ++s) {
            if (s == 1) MARK(1);
            if (w < 4) {
                const unsigned m0 = comm + cmap(C_MEM + s * 10240 + w * 1024, nearbit), m1 = comm + cmap(C_MEM + s * 10240 + (4 + w) * 1024, nearbit), m2 = comm + cmap(C_MEM + s * 10240 + (8 + (w & 1)) * 1024, nearbit);
                const unsigned ml = lb + L_MEM + w * 1024;
                if (s) wait_rt((w == 3 ? newest_stamp(rw, vopaque(l16 < 512 ? l16 * 8 : 0x80000000u) + fh, s, alive, lb + L_T) : lds_stamp(s, lb + L_T)) + CU + CH);
                for (;;) {
                    dma_c<0>(sw, l16, m0, ml); dma_c<0>(sw, l16, m1, ml + 4096);
                    if (w < 2) dma_c<0>(sw, l16, m2, ml + 8192);
                    VMCNT(0);
                    const u32x4 c0 = lds16(ml + l16), c1 = lds16(ml + 4096 + l16), c2 = lds16(ml + (w < 2 ? 8192 : 0) + l16);
                    const unsigned mx = max3u(max3u(max3u(c0.x, c0.y, c0.z), c0.w, c1.x), max3u(max3u(c1.y, c1.z, c1.w), c2.x, c2.y), max(c2.z, c2.w));
                    if (__builtin_expect(__builtin_amdgcn_ballot_w64(mx == ~0u) == 0 || nretry > RETRY_CAP, 1)) break;
                    ++nretry;
                    __builtin_amdgcn_s_sleep(BKH);
                }
            } else VMCNT(0);
            const unsigned kq = lb + L_KQ + (s & 1) * 16384;
            barrier();
            if (t == 4 * 64) st8c(rw, (u32x2){(unsigned)s + 1, rtc()}, 0, fh + h * 128);
            u32x4 a[10];
#pragma unroll
            for (int k = 0; k < 8; ++k) a[k] = lds16(lb + L_MEM + k * 1024 + l16);
            SB();
            if (w >= 4 && s && s + 1 < NSEG) prefetch(s + 1);
            f32x4 q = {0, 0, 0, 0}, kk = q;
            SB();
#pragma unroll
            for (int k = 0; k < 10; ++k) { q = smfma(a[k], wq[k], q, idx); SB(); kk = smfma(a[k], wk[k], kk, idx); if (k < 2) a[8 + k] = lds16(lb + L_MEM + (8 + k) * 1024 + l16); SB(); }
            u32x8 kx[4];
            if (w < 3) {
#pragma unroll
                for (int k = 0; k < 4; ++k) { const u32x4 x = lds16(kq + l16 + k * 2048), y = lds16(kq + l16 + k * 2048 + 1024); kx[k] = (u32x8){x.x, x.y, x.z, x.w, y.x, y.y, y.z, y.w}; }
            } else {
#pragma unroll
                for (int k = 0; k < 4; ++k) asm volatile("" : "=v"(kx[k]));
            }
            lds4w(lb + L_QI + ia, pk2v(halfsum2(q)));
            lds4w(lb + L_KI + ia, pk2v(halfsum2(kk)));
            barrier();
            if (w == 0) {
                u32x2 qr[4], kr[8];
#pragma unroll
                for (int k = 0; k < 4; ++k) qr[k] = ldstr(lb + L_QI + (k >> 1) * 1024 + (k & 1) * 64 + trq);
#pragma unroll
                for (int k = 0; k < 8; ++k) kr[k] = ldstr(lb + L_KI + (k >> 1) * 512 + (k & 1) * 128 + tra);
                SB();
                const u32x4 qa0 = {qr[0].x, qr[0].y, qr[1].x, qr[1].y}, qa1 = {qr[2].x, qr[2].y, qr[3].x, qr[3].y};
                f32x4 d0 = {0, 0, 0, 0}, d1 = d0, dm = d0;
                d0 = smfma(qa0, kx[0], d0, idx); d1 = smfma(qa0, kx[2], d1, idx);
                d0 = smfma(qa1, kx[1], d0, idx); d1 = smfma(qa1, kx[3], d1, idx);
                dm = smfma(qa0, (u32x8){kr[0].x, kr[0].y, kr[1].x, kr[1].y, kr[2].x, kr[2].y, kr[3].x, kr[3].y}, dm, idx);
                dm = smfma(qa1, (u32x8){kr[4].x, kr[4].y, kr[5].x, kr[5].y, kr[6].x, kr[6].y, kr[7].x, kr[7].y}, dm, idx);
                const f32x2 s0 = halfsum2(d0), s1 = halfsum2(d1), sm = halfsum2(dm) + (f32x2){mneg, mneg};
                float mxa = mx3(s0[0], s1[0], sm[0]), mxb = mx3(s0[1], s1[1], sm[1]);
                ROW16("v_max_f32", mxa, mxb);
                const f32x2 cc = {SC_LOG2E, SC_LOG2E}, mm = (f32x2){mxa, mxb} * cc;
                const f32x2 e0 = ex2(s0 * cc - mm), e1 = ex2(s1 * cc - mm), em = ex2(sm * cc - mm);
                const f32x2 tt = e0 + e1 + em;
                float sa = tt[0], sb = tt[1];
                ROW16("v_add_f32", sa, sb);
                const bool pos = (s / PRING) & 1;
                const float sgn = pos ? 1.f : -1.f;
                const f32x2 iv = (f32x2){__builtin_amdgcn_rcpf(sa), __builtin_amdgcn_rcpf(sb)} * (f32x2){sgn, sgn};
                const unsigned tag = pos ? 0u : 0x80008000u, pb = comm + cmap(C_P + (s % PRING) * 32768 + (h >> 2) * 4096, nearbit);
                if (s < NSEG - 1) {
                    st4c(rw, (pk2v(e0 * iv) & vmask) | tag, po, pb); st4c(rw, (pk2v(e1 * iv) & vmask) | tag, po + 256, pb);
                    st4c(rw, (pk2v(em * iv) & vmask) | tag, pm_o, pb);
                }
            } else if (w < 3) {
                const int qt = w - 1;
                f32x4 sx0 = {0, 0, 0, 0}, sx1 = sx0, sm = sx0;
#pragma unroll
                for (int k = 0; k < 4; ++k) {
                    const bf16x8 qx = bf8(lds16(kq + 8192 + qt * 4096 + k * 1024 + l16));
                    const u32x8 ka = kx[k >> 1], kb = kx[2 + (k >> 1)];
                    sx0 = mfma16(bf8((k & 1) ? (u32x4){ka[4], ka[5], ka[6], ka[7]} : (u32x4){ka[0], ka[1], ka[2], ka[3]}), qx, sx0);
                    sx1 = mfma16(bf8((k & 1) ? (u32x4){kb[4], kb[5], kb[6], kb[7]} : (u32x4){kb[0], kb[1], kb[2], kb[3]}), qx, sx1);
                    sm = mfma16(bf8(ldstr(lb + L_KI + k * 512 + tra), ldstr(lb + L_KI + k * 512 + 128 + tra)), qx, sm);
                    asm volatile("" : "+v"(sm)); SB();
                }
                const int l2 = vopaque(l16) >> 4, gv = l2 >> 4, cv = l2 & 15;
                const int lim = 16 * qt + cv - (16 * (gv >> 1) + 4 * (gv & 1));
#pragma unroll
                for (int r = 0; r < 4; ++r) { if (r > lim) sx0[r] = NEG; if (r + 8 > lim) sx1[r] = NEG; }
                if (gv >= 2) sm = (f32x4){NEG, NEG, NEG, NEG};
                u32x4 px; u32x2 pm;
                softmax40(sx0, sx1, sm, px, pm);
                const unsigned base = WS_PX + (s * 32 + h) * 2048;
                if (qt == 1 || gv < 2) st16c(rw, px, (1 - qt) * 1024 + l16, base);
                if (gv < 2) st8c(rw, pm, 1536 + qt * 256 + cv * 16 + gv * 8, base);
            } else if (w >= 4 && (have < s + 3 || !s) && s + 2 < NSEG) {
                __builtin_amdgcn_s_sleep(PF_SLEEP);
                if (!s) { wait_prog<2>(rw, pg(layer, PG_QK + h), 2, have); prefetch(1); }
                wait_prog<2>(rw, pg(layer, PG_QK + h), s + 3, have);
            }
        }
        VMCNT(0); barrier();
        if (t == 0) st4c(rw, NSEG + 1, 0, fh + h * 128);
        if (lane == 0 && nretry > RETRY_CAP) st4c(rw, 1, WS_CTRL + CT_ABORT * 4, 0);
        MARK(2);
        phase_mix2(layer, lb);
    }
}

DI void role_u(int u, unsigned lb) {
    const Ctx c = args();
    const int t = threadIdx.x, lane = t & 63, w = rfl(t >> 6);
    constexpr unsigned L_MEM = 0, L_P = 10240  , L_UX = L_P + 20480  , L_R = L_UX + 32768  , L_T = L_R + 5120, L_UM = L_T + 64  ;
    const u32x4 sw = mk_srd(c.ws, WS_END);
    const __amdgpu_buffer_rsrc_t rw = mk_rs(c.ws, WS_END);
    u32x8 wb[2][10];
    {
        const char* src = c.ws + WS_BF + (size_t)(u * 8 + w) * 40960 + lane * 16;
#pragma unroll
        for (int tt = 0; tt < 2; ++tt)
#pragma unroll
            for (int k = 0; k < 10; ++k) {
                const u32x4 x = *(const u32x4*)(src + (tt * 10 + k) * 2048), y = *(const u32x4*)(src + (tt * 10 + k) * 2048 + 1024);
                wb[tt][k] = (u32x8){x.x, x.y, x.z, x.w, y.x, y.y, y.z, y.w};
            }
    }
    for (int layer = 0; layer < 2; ++layer) {
        MARK(0);
        const int lv = vopaque(lane), g = lv >> 4, col = lv & 15;
        const unsigned comm = WS_COMM + layer * 2 * MB;
        const unsigned nearbit = sopaque(*(const unsigned*)(c.ws + WS_CTRL + layer * 4096 + CT_NEAR * 4));
        const unsigned pl = lb + L_P + w * 2560, l16 = lv * 16;
        const int idx = (lv & 8) ? 0xEEEE : 0x4444;
        unsigned have = 0, nr1 = 0, nr3 = 0, alive = ~0u;
        const unsigned pvo = pg(layer, PG_VU + (u >= 40 ? 32 : 0) + (lv & 31));
        const bool diag = (col >> 3) == (g >> 1);
        const unsigned ra = lb + L_R + ((col & 7) * 2 + (g & 1)) * 80 + (w * 2 + (g >> 1)) * 4;
        auto ux_dma = [&](int s) {
            const unsigned src = WS_UX + ux_slot(u, s) + (w - 6) * 8192, dst = lb + L_UX + (s & 1) * 16384 + (w - 6) * 8192;
            dma_c<0>(sw, l16, src, dst); dma_c<1024>(sw, l16, src, dst); dma_c<2048>(sw, l16, src, dst); dma_c<3072>(sw, l16, src, dst);
            dma_c<0>(sw, l16, src + 4096, dst + 4096); dma_c<1024>(sw, l16, src + 4096, dst + 4096); dma_c<2048>(sw, l16, src + 4096, dst + 4096); dma_c<3072>(sw, l16, src + 4096, dst + 4096);
        };
        auto um_store = [&](int s) {
            const unsigned o = (w - 6) * 2048 + l16;
            const unsigned l = lb + L_UM + (s & 1) * 4096 + o;
            st16c(rw, lds16(l), o, WS_UM + ux_slot(u, s) / 4); st16c(rw, lds16(l + 1024), o + 1024, WS_UM + ux_slot(u, s) / 4);
        };
        u32x4 ux0, ux1;
        u32x2 um;
        const unsigned vf = vopaque(lv < 32 ? lv * 128 : 0x80000000u) + comm + cmap(C_FH, nearbit);
        if (t == 0) lds4w(lb + L_T, 0);
        auto um_calc = [&](int s) {
            if (w < 5) {
                const unsigned ml = lb + L_MEM + w * 1024, m0 = comm + cmap(C_MEM + s * 10240 + w * 1024, nearbit), m1 = comm + cmap(C_MEM + s * 10240 + (w + 5) * 1024, nearbit);
                for (;;) {
                    dma_c<0>(sw, l16, m0, ml); dma_c<0>(sw, l16, m1, ml + 5120);
                    VMCNT(0);
                    const u32x4 c0 = lds16(ml + l16), c1 = lds16(ml + 5120 + l16);
                    if (__builtin_amdgcn_ballot_w64(max3u(max3u(max3u(c0.x, c0.y, c0.z), c0.w, c1.x), max3u(c1.y, c1.z, c1.w), 0) == ~0u) == 0 || nr3 > RETRY_CAP) break;
                    ++nr3;
                    __builtin_amdgcn_s_sleep(BKU);
                }
            } else { if (w >= 6 && s && s + 1 < NSEG - 1) wait_prog<2>(rw, pvo, s + 2, have); VMCNT(0); }
            barrier();
            if (t == 6 * 64) st4c(rw, s, 0, pg(layer, PG_U + u));
            f32x4 e0 = {0, 0, 0, 0}, e1 = e0;
            u32x4 a[10];
#pragma unroll
            for (int k = 0; k < 10; ++k) a[k] = lds16(lb + L_MEM + k * 1024 + l16);
            SB();
#pragma unroll
            for (int k = 0; k < 10; ++k) { e0 = smfma(a[k], wb[0][k], e0, idx); SB(); e1 = smfma(a[k], wb[1][k], e1, idx); SB(); }
            ux0 = lds16(lb + L_UX + (s & 1) * 16384 + w * 2048 + l16); ux1 = lds16(lb + L_UX + (s & 1) * 16384 + w * 2048 + 1024 + l16);
            um = (u32x2){pk2(halfsum(e0[0], e1[0]), halfsum(e0[1], e1[1])), pk2(halfsum(e0[2], e1[2]), halfsum(e0[3], e1[3]))};
            *(LAS u32x2*)(lb + L_UM + (s & 1) * 4096 + w * 512 + ((2 * (g >> 1) + (col >> 3)) * 8 + (col & 7)) * 16 + (g & 1) * 8) = um;
        };
        if (w >= 6) { wait_prog<2>(rw, pvo, 1, have); ux_dma(0); }
        um_calc(0);
        const unsigned pt = pl + ((lv >> 1) & 1) * 640 + (4 * g + ((lv >> 2) & 3)) * 16 + (lv & 1) * 8;
        const unsigned ptm = pl + ((g >> 1) * 2 + ((lv >> 1) & 1)) * 640 + 512 + (4 * (g & 1) + ((lv >> 2) & 3)) * 16 + (lv & 1) * 8;
        MARK(1);
        for (int s = 0; s < NSEG - 1; ++s) {
            const unsigned psrc = comm + cmap(C_P + (s % PRING) * 32768 + w * 4096, nearbit);
            const unsigned go = (w == 3 ? newest_stamp(rw, vf, s + 1, alive, lb + L_T) : lds_stamp(s + 1, lb + L_T)) + CU;
            wait_rt(go);
            dma_c<0>(sw, l16, psrc, pl); dma_c<1024>(sw, l16, psrc, pl);
            if (lv < 32) dma_c<2048>(sw, l16, psrc, pl);
            if (w >= 6 && s > 0 && s + 1 < NSEG - 1) {
                __builtin_amdgcn_s_sleep(2);
                ux_dma(s + 1);
                VMCNT(8);
            } else VMCNT(0);
            const bool pos = (s / PRING) & 1;
            const unsigned tag = pos ? 0u : 0x80008000u;
            for (;;) {
                const u32x2 p0 = ldstr(pt), p1 = ldstr(pt + 256), p2 = ldstr(pt + 1280), p3 = ldstr(pt + 1536), pm = ldstr(ptm);
                f32x4 acc = mfma16(bf8(ux0), bf8(p0, p1), (f32x4){0, 0, 0, 0});
                acc = mfma16(bf8(ux1), bf8(p2, p3), acc);
                acc = mfma16k16(um, pm, acc);
                const bool bad = ((((p0.x ^ tag) | (p0.y ^ tag)) | ((p1.x ^ tag) | (p1.y ^ tag)) | ((p2.x ^ tag) | (p2.y ^ tag)) | ((p3.x ^ tag) | (p3.y ^ tag)) | ((pm.x ^ tag) | (pm.y ^ tag))) & 0x80008000u) != 0;
                if (diag) {
#pragma unroll
                    for (int r = 0; r < 4; ++r) lds4w(ra + r * 1280, __float_as_uint(acc[r]));
                }
                if (__builtin_expect(__builtin_amdgcn_ballot_w64(bad) == 0 || nr1 > RETRY_CAP, 1)) break;
                ++nr1;
                dma_c<0>(sw, l16, psrc, pl); dma_c<1024>(sw, l16, psrc, pl);
                if (lv < 32) dma_c<2048>(sw, l16, psrc, pl);
                VMCNT(0);
            }
            barrier();
            if (w == 0) {
                const unsigned ad = lb + L_R + lv * 80;
                const f32x4 v = (__builtin_bit_cast(f32x4, lds16(ad)) + __builtin_bit_cast(f32x4, lds16(ad + 16))) + (__builtin_bit_cast(f32x4, lds16(ad + 32)) + __builtin_bit_cast(f32x4, lds16(ad + 48)));
                const f32x2 v2 = lo2(v) + hi2(v);
                float x = v2[0] + v2[1];
                if (!pos) x = -x;
                auto r = __builtin_amdgcn_permlane16_swap(__float_as_uint(x), __float_as_uint(x), false, false);
                const unsigned o = min(pk2(__uint_as_float(r[0]), __uint_as_float(r[1])), 0xFFFFFFFEu);
                if (!(g & 1)) st4c(rw, o, col * 8 + (g >> 1) * 4, comm + cmap(C_MEM + (s + 1) * 10240 + u * 128, nearbit));
            } else if (w >= 6) {
                __builtin_amdgcn_s_sleep(5); um_store(s);
                if (!s) { wait_prog<2>(rw, pvo, 2, have); ux_dma(1); }
            }
            if (w < 5) wait_rt(go + CH);
            um_calc(s + 1);
        }
        barrier();
        if (w >= 6) um_store(NSEG - 1);
        VMCNT(0); barrier();
        if (t == 0) st4c(rw, NSEG, 0, pg(layer, PG_U + u));
        if (lane == 0 && (nr1 > RETRY_CAP || nr3 > RETRY_CAP)) st4c(rw, 1, WS_CTRL + CT_ABORT * 4, 0);
        MARK(2);
        phase_mix2(layer, lb);
    }
}

DI void role_x(int x, unsigned lb) {
    const Ctx c = args();
    const int t = threadIdx.x, lane = t & 63, w = rfl(t >> 6), r = lane & 31, hf = lane >> 5, cg = x >> 1;
    constexpr unsigned WV = 18944, O_UX = 8192  , O_UM = O_UX + 8704  , L_Z = 8 * WV;
    const u32x4 sw = mk_srd(c.ws, WS_END);
    const __amdgpu_buffer_rsrc_t rw = mk_rs(c.ws, WS_END);
    const unsigned wl = lb + w * WV, l16 = lane * 16;
    for (int layer = 0; layer < 2; ++layer) {
        MARK(0);
        const __amdgpu_buffer_rsrc_t rx = layer ? mk_rs(c.ws + WS_X2, S * D * 2) : mk_rs(c.x, S * D * 2);
        const unsigned pv1 = pg(layer, PG_VU + (cg >= 10 ? 32 : 0) + r), pv2 = pg(layer, PG_R + (blockIdx.x & 7));
        unsigned have1 = 0, have2 = 0;
        if (t == 0) lds16w(lb + L_Z, (u32x4){0, 0, 0, 0});
        auto ux_dma = [&](int s) {
            wait_prog<8>(rw, pv1, s + 1, have1);
#pragma unroll
            for (int i = 0; i < 4; ++i) {
                const unsigned src = WS_UX + sopaque(ux_slot(4 * cg + i, s)) + w * 2048;
                asm volatile("s_nop 3");
                dma_c<0>(sw, l16, src, wl + O_UX + i * 2176); dma_c<1024>(sw, l16, src, wl + O_UX + i * 2176);
            }
        };
#define XP(i)
        ux_dma(x & 1);
        for (int s = x & 1; s < NSEG; s += 2) {
            if (w == 7) wait_prog<XSLEEP>(rw, pv2, s + 1, have2);
            barrier();
            if (s >= NSEG - 2) MARK(3);
            XP(0);
            {
                const unsigned src = WS_PX + (s * 32 + w * 4) * 2048;
                dma_c<0>(sw, l16, src, wl); dma_c<1024>(sw, l16, src, wl); dma_c<2048>(sw, l16, src, wl); dma_c<3072>(sw, l16, src, wl);
                dma_c<0>(sw, l16, src + 4096, wl + 4096); dma_c<1024>(sw, l16, src + 4096, wl + 4096); dma_c<2048>(sw, l16, src + 4096, wl + 4096); dma_c<3072>(sw, l16, src + 4096, wl + 4096);
                dma_c<0>(sw, ux_slot(4 * cg + hf, s) / 4 + r * 16, WS_UM + w * 512, wl + O_UM);
                dma_c<0>(sw, ux_slot(4 * cg + 2 + hf, s) / 4 + r * 16, WS_UM + w * 512, wl + O_UM + 1024);
            }
            const unsigned oo = ((s * 32 + r) * D + cg * 32 + (w & 3) * 8 + hf * 4) * 2;
            u32x2 xr = {0, 0};
            if (w < 4) xr = ld8c(rx, oo, 0);
            VMCNT(0);
            XP(1);
            f32x16 acc = {};
            const unsigned ub = wl + O_UX + (r >> 3) * 2176 + (r & 7) * 16 + hf * 256, pb0 = wl + (r < 16 ? 1024 : 0) + hf * 256 + (r & 15) * 16, pb1 = r < 16 ? lb + L_Z : pb0 + 512;
            u32x4 fa[10], fb[10];
#pragma unroll
            for (int hh = 0; hh < 4; ++hh) {
                fa[2 * hh] = lds16(ub + (hh >> 1) * 1024 + (hh & 1) * 128); fb[2 * hh] = lds16(pb0 + hh * 2048);
                fa[2 * hh + 1] = lds16(ub + (hh >> 1) * 1024 + (hh & 1) * 128 + 512); fb[2 * hh + 1] = lds16(pb1 + (r < 16 ? 0 : hh * 2048));
            }
#pragma unroll
            for (int i = 0; i < 2; ++i) { fa[8 + i] = lds16(wl + O_UM + (r >> 3) * 512 + (2 * i + hf) * 128 + (r & 7) * 16); fb[8 + i] = lds16(wl + (2 * i + hf) * 2048 + 1536 + r * 16); }
            SB();
#pragma unroll
            for (int i = 0; i < 10; ++i) acc = mfma32(bf8(fa[i]), bf8(fb[i]), acc);
#pragma unroll
            for (int i = 0; i < 4; ++i) lds16w(wl + i * 1024 + l16, __builtin_bit_cast(u32x4, (f32x4){acc[4 * i], acc[4 * i + 1], acc[4 * i + 2], acc[4 * i + 3]}));
            barrier();
            if (w < 4) {
                f32x4 v = {0, 0, 0, 0};
#pragma unroll
                for (int i = 0; i < 8; ++i) v += __builtin_bit_cast(f32x4, lds16(lb + i * WV + w * 1024 + l16));
                st8c(rw, (u32x2){pk2(blo(xr.x) + rbf(v[0]), bhi(xr.x) + rbf(v[1])), pk2(blo(xr.y) + rbf(v[2]), bhi(xr.y) + rbf(v[3]))}, oo, WS_X1);
                VMCNT(0);
            }
            barrier();
            XP(2);
            if (t == 0) st4c(rw, (s >> 1) + 1, 0, pg(layer, PG_X + x));
            if (s + 2 < NSEG) ux_dma(s + 2);
            XP(3);
        }
        MARK(4);
        phase_mix2(layer, lb);
    }
}

DI void role_ln(int j, unsigned lb) {
    const Ctx c = args();
    const int t = threadIdx.x, lane = t & 63, w = rfl(t >> 6), row = t >> 4, i16 = t & 15;
    const __amdgpu_buffer_rsrc_t rw = mk_rs(c.ws, WS_END);
    for (int layer = 0; layer < 2; ++layer) {
        if (j == 7 && w == 0) {
            const unsigned fh = WS_COMM + layer * 2 * MB + cmap(C_FH, *(const unsigned*)(c.ws + WS_CTRL + layer * 4096 + CT_NEAR * 4));
            const unsigned v0 = lane < 32 ? fh + lane * 128 : pg(layer, PG_U + 64 + (lane & 15)), v1 = pg(layer, PG_U + lane);
            unsigned done = 0;
            for (unsigned n = 0; done < NSEG && n < RETRY_CAP * 16; ++n) {
                asm volatile("" ::: "memory");
                const unsigned a = ld4c(rw, v0, 0), b = ld4c(rw, v1, 0);
                const unsigned m = wave_min(min(lane < 32 ? max(a, 1u) - 1 : a, b));
                if (m > done) { done = m; if (lane < 8) st4c(rw, m, pg(layer, PG_R + lane), 0); if (m < NSEG - 1) __builtin_amdgcn_s_sleep(AGG1); }
                else __builtin_amdgcn_s_sleep(AGG2);
            }
        }
        for (int s = j < 7 ? j : NSEG; s < NSEG; s += 7) {
            if (w == 7) { unsigned have = 0; wait_prog<LSLEEP>(rw, pg(layer, PG_X + (lane % 20) * 2 + (s & 1)), (s >> 1) + 1, have); }
            barrier();
            float v[40];
#pragma unroll
            for (int i = 0; i < 5; ++i) unpack8(ld16c(rw, (s * 32 + row) * D * 2 + (i16 + 16 * i) * 16, WS_X1), v + 8 * i);
            ln40(v); ln40(v);
#pragma unroll
            for (int i = 0; i < 5; ++i) lds16w(lb + row * 1312 + (i16 + 16 * i) * 16, pack8(v + 8 * i));
            barrier();
            const unsigned ad = lb + (4 * (lane >> 4) + ((lane >> 2) & 3)) * 1312 + (lane & 3) * 8 + w * 160;
#pragma unroll
            for (int i = 0; i < 5; ++i) { const u32x2 a = ldstr(ad + i * 32), b = ldstr(ad + i * 32 + 16 * 1312); st16c(rw, (u32x4){a.x, a.y, b.x, b.y}, lane * 16 + i * 1024, WS_H3 + s * 40960 + w * 5120); }
            VMCNT(0); barrier();
            if (t == 0) st4c(rw, s / 7 + 1, 0, pg(layer, PG_L + j));
        }
        MARK(5);
        phase_mix2(layer, lb);
    }
}

DI void h_dma(__amdgpu_buffer_rsrc_t rw, u32x4 sw, int layer, int s, int w, unsigned lane, unsigned lb, unsigned& have) {
    if (layer) for (unsigned n = 0; have <= (unsigned)s; ++n) {
        asm volatile("" ::: "memory");
        const unsigned long long m = ~__builtin_amdgcn_ballot_w64(ld4c(rw, min(s * 4 + lane, 255u) * 128, pg(0, PG_HF)) != 0);
        have = s + (m ? __builtin_ctzll(m) : 64) / 4;
        if (n > RETRY_CAP * 4) { st4c(rw, 1, WS_CTRL + CT_ABORT * 4, 0); have = 1000; }
    }
    const unsigned src = WS_HF + s * 40960 + w * 5120, dst = lb + (s % 3) * 40960 + w * 5120, l16 = lane * 16;
    dma_c<0>(sw, l16, src, dst); dma_c<1024>(sw, l16, src, dst); dma_c<2048>(sw, l16, src, dst); dma_c<3072>(sw, l16, src, dst);
    dma_c<0>(sw, l16, src + 4096, dst + 4096);
}
DI u32x4 pk8(const f32x16& a, int o) { return (u32x4){pk2(a[o], a[o + 1]), pk2(a[o + 2], a[o + 3]), pk2(a[o + 4], a[o + 5]), pk2(a[o + 6], a[o + 7])}; }

DI void role_qk(int h, int fb, int db, unsigned lb) {
    const Ctx c = args();
    const int t = threadIdx.x, lane = t & 63, w = rfl(t >> 6), et = w & 3, mat = w >> 2;
    const u32x4 sw = mk_srd(c.ws, WS_END);
    const __amdgpu_buffer_rsrc_t rw = mk_rs(c.ws, WS_END);
    u32x4 w1[40];
    {
        const u32x4 s1 = mk_srd(mat ? c.Wk : c.Wq, D * HD * 2);
        const unsigned vo = (lane >> 4) * 8192 + h * 256 + (lane & 15) * 16;
        auto stage = [&](int st) {
#pragma unroll
            for (int i = 0; i < 10; ++i) dma_p<0>(s1, vo, (st * 160 + (et * 10 + i) * 4) * 8192, lb + (st & 1) * 81920 + (w * 10 + i) * 1024);
        };
        stage(0);
#pragma unroll
        for (int st = 0; st < 4; ++st) {
            if (st < 3) { stage(st + 1); VMCNT(10); } else VMCNT(0);
            barrier();
#pragma unroll
            for (int k = 0; k < 10; ++k) {
                const unsigned ad = lb + (st & 1) * 81920 + (mat * 160 + 16 * k + 8 * (lane >> 5) + ((lane >> 2) & 3)) * 256 + (et * 32 + 16 * ((lane >> 4) & 1) + 4 * (lane & 3)) * 2;
                const u32x2 x = ldstr(ad), y = ldstr(ad + 1024);
                w1[st * 10 + k] = (u32x4){x.x, x.y, y.x, y.y};
            }
            barrier();
        }
    }
    for (int layer = 0; layer < 2; ++layer) {
        MARK(0);
        const unsigned lv = vopaque(lane), r = lv & 31, hf = lv >> 5, pr = pg(layer, PG_QK + h);
        unsigned have = 0;
        barrier();
        h_dma(rw, sw, layer, 0, w, lv, lb, have); h_dma(rw, sw, layer, 1, w, lv, lb, have); VMCNT(5); barrier();
        MARK(1);
        const unsigned qko = mat == 0 ? (r >> 4) * 4096 + et * 1024 + (hf * 16 + (r & 15)) * 16 : ((r >> 3) & 1) * 4096 + et * 1024 + (hf * 16 + 8 * (r >> 4) + 4 * ((r >> 2) & 1) + (r & 3)) * 16;
        for (int s = 0; s < NSEG; ++s) {
            if (s + 2 < NSEG) h_dma(rw, sw, layer, s + 2, w, lv, lb, have);
            f32x16 acc = {};
            const unsigned hb = lb + (s % 3) * 40960 + lv * 16;
            u32x4 b[8];
#pragma unroll
            for (int k = 0; k < 8; ++k) b[k] = lds16(hb + k * 1024);
            SB();
#pragma unroll
            for (int k = 0; k < 40; ++k) { acc = mfma32(bf8(w1[k]), bf8(b[k & 7]), acc); if (k + 8 < 40) b[k & 7] = lds16(hb + (k + 8) * 1024); SB(); }
            if (s + 2 < NSEG) VMCNT(5); else VMCNT(0);
            barrier();
            if (t == 0 && s > 2) st4c(rw, s, 0, pr);
            const unsigned dst = (mat == 0 ? WS_QF : WS_KF) + qk_slot(h, s);
            st16c(rw, pk8(acc, 0), qko, dst); st16c(rw, pk8(acc, 8), qko + 512, dst);
            if (s < 2 || s == NSEG - 1) { VMCNT(0); barrier(); if (t == 0) st4c(rw, s + 1, 0, pr); }
        }
        MARK(2);
        phase_mix1(layer, fb, db, lb);
        phase_mix2(layer, lb);
    }
}

DI void role_vu(int ch, int h, int fb, int db, unsigned lb) {
    const Ctx c = args();
    const int t = threadIdx.x, lane = t & 63, w = rfl(t >> 6), et = w & 3, kh = w >> 2;
    constexpr unsigned L_EX = 122880  , L_V = L_EX + 16384;
    const u32x4 sw = mk_srd(c.ws, WS_END);
    const __amdgpu_buffer_rsrc_t rw = mk_rs(c.ws, WS_END);
    u32x4 w1[20], w2[2][8];
    {
        const int q = (lane >> 2) & 3, p = lane & 3, c16 = (lane >> 4) & 1, hf = lane >> 5;
        const u32x4 s1 = mk_srd(c.Wv, D * HD * 2);
        const unsigned vo = (lane >> 4) * 8192 + h * 256 + (lane & 15) * 16;
#pragma unroll
        for (int st = 0; st < 2; ++st)
#pragma unroll
            for (int i = 0; i < 10; ++i) dma_p<0>(s1, vo, (kh * 320 + st * 160 + (et * 10 + i) * 4) * 8192, lb + st * 81920 + (w * 10 + i) * 1024);
#pragma unroll
        for (int st = 0; st < 2; ++st) {
            if (st == 0) VMCNT(10); else VMCNT(0);
            barrier();
#pragma unroll
            for (int k = 0; k < 10; ++k) {
                const unsigned ad = lb + st * 81920 + (kh * 160 + 16 * k + 8 * hf + q) * 256 + (et * 32 + 16 * c16 + 4 * p) * 2;
                const u32x2 x = ldstr(ad), y = ldstr(ad + 1024);
                w1[st * 10 + k] = (u32x4){x.x, x.y, y.x, y.y};
            }
            if (st == 0) {
                barrier();
                const u32x4 so = mk_srd(c.Wo, HD * D * 2);
#pragma unroll
                for (int i = 0; i < 10; ++i) {
                    const unsigned by = (w * 10 + i) * 1024 + lane * 16;
                    dma_p<0>(so, (h * 128 + by / 640) * 1280 + ch * 640 + by % 640, 0, lb + (w * 10 + i) * 1024);
                }
            }
        }
#pragma unroll
        for (int tt = 0; tt < 2; ++tt)
#pragma unroll
            for (int k = 0; k < 8; ++k) {
                const int ct = tt == 0 ? w : 8 + (w & 1);
                const unsigned ad = lb + (16 * k + 4 * hf + q) * 640 + (ct * 32 + 16 * c16 + 4 * p) * 2;
                const u32x2 x = ldstr(ad), y = ldstr(ad + 8 * 640);
                w2[tt][k] = (u32x4){x.x, x.y, y.x, y.y};
            }
    }
    for (int layer = 0; layer < 2; ++layer) {
        MARK(0);
        const unsigned lv = vopaque(lane), r = lv & 31, hf = lv >> 5, l16 = lv * 16, pr = pg(layer, PG_VU + ch * 32 + h);
        unsigned have = 0;
        barrier();
        h_dma(rw, sw, layer, 0, w, lv, lb, have); h_dma(rw, sw, layer, 1, w, lv, lb, have); VMCNT(5); barrier();
        MARK(1);
        for (int s = 0; s < NSEG; ++s) {
            if (s + 2 < NSEG) h_dma(rw, sw, layer, s + 2, w, lv, lb, have);
            f32x16 acc = {};
            const unsigned hb = lb + (s % 3) * 40960 + kh * 20480 + l16;
            u32x4 b[8];
#pragma unroll
            for (int k = 0; k < 8; ++k) b[k] = lds16(hb + k * 1024);
            SB();
#pragma unroll
            for (int k = 0; k < 20; ++k) { acc = mfma32(bf8(w1[k]), bf8(b[k & 7]), acc); if (k + 8 < 20) b[k & 7] = lds16(hb + (k + 8) * 1024); SB(); }
            const unsigned ex = lb + L_EX;
            f32x4 m0, m1;
            if (kh == 0) {
                lds16w(ex + w * 2048 + l16, __builtin_bit_cast(u32x4, (f32x4){acc[8], acc[9], acc[10], acc[11]}));
                lds16w(ex + w * 2048 + 1024 + l16, __builtin_bit_cast(u32x4, (f32x4){acc[12], acc[13], acc[14], acc[15]}));
                m0 = (f32x4){acc[0], acc[1], acc[2], acc[3]}; m1 = (f32x4){acc[4], acc[5], acc[6], acc[7]};
            } else {
                lds16w(ex + w * 2048 + l16, __builtin_bit_cast(u32x4, (f32x4){acc[0], acc[1], acc[2], acc[3]}));
                lds16w(ex + w * 2048 + 1024 + l16, __builtin_bit_cast(u32x4, (f32x4){acc[4], acc[5], acc[6], acc[7]}));
                m0 = (f32x4){acc[8], acc[9], acc[10], acc[11]}; m1 = (f32x4){acc[12], acc[13], acc[14], acc[15]};
            }
            if (s + 2 < NSEG) VMCNT(5); else VMCNT(0);
            barrier();
            if (t == 0 && s > 2) st4c(rw, s, 0, pr);
            m0 += __builtin_bit_cast(f32x4, lds16(ex + (w ^ 4) * 2048 + l16));
            m1 += __builtin_bit_cast(f32x4, lds16(ex + (w ^ 4) * 2048 + 1024 + l16));
            lds16w(lb + L_V + (2 * et + kh) * 1024 + l16, (u32x4){pk2(m0[0], m0[1]), pk2(m0[2], m0[3]), pk2(m1[0], m1[1]), pk2(m1[2], m1[3])});
            barrier();
            bf16x8 v[8];
#pragma unroll
            for (int k = 0; k < 8; ++k) v[k] = bf8(lds16(lb + L_V + k * 1024 + l16));
#pragma unroll
            for (int tt = 0; tt < 2; ++tt) {
                if (tt == 1 && w >= 2) break;
                f32x16 a2 = {};
#pragma unroll
                for (int k = 0; k < 8; ++k) a2 = mfma32(v[k], bf8(w2[tt][k]), a2);
                const int col = ch * 320 + (tt == 0 ? w : 8 + (w & 1)) * 32 + r;
                const unsigned vo = ux_slot(col >> 3, s) + (h >> 1) * 1024 + (hf * 16 + (h & 1) * 8 + (col & 7)) * 16;
                st16c(rw, pk8(a2, 0), vo, WS_UX); st16c(rw, pk8(a2, 8), vo + 512, WS_UX);
            }
            if (s < 2 || s == NSEG - 1) { VMCNT(0); barrier(); if (t == 0) st4c(rw, s + 1, 0, pr); }
        }
        MARK(2);
        phase_mix1(layer, fb, db, lb);
        phase_mix2(layer, lb);
    }
}

__global__ void __launch_bounds__(512, 2) k_main(const u16* x, const u16* Wq, const u16* Wk, const u16* Wv, const u16* Wo, const u16* w1, const u16* b1, const u16* w2, const u16* b2, u16* out, char* ws) {
    extern __shared__ __attribute__((aligned(16))) char smem[];
    const unsigned lb = (unsigned)(size_t)(LAS char*)smem;
    const int xcd = blockIdx.x & 7, i = blockIdx.x >> 3, j = i * 3 + xcd - 1, y = xcd - 4;
    if (xcd == 0) role_head(i, lb);
    else if (xcd < 4 && j < 80) role_u(j, lb);
    else if (xcd < 4 && j >= 88) role_ln(j - 88, lb);
    else if (xcd < 4 || i >= 24) role_x(xcd < 4 ? j - 48 : y * 8 + i - 24, lb);
    else if (i < 8) role_qk(y * 8 + i, y * 3, i, lb);
    else role_vu(i & 1, y * 8 + ((i - 8) >> 1), y * 3 + (i >> 3), i & 7, lb);
}

extern "C" void kernel_launch(void* const* d_in, const int* in_sizes, int n_in, void* d_out, int out_size, void* d_ws, size_t ws_size, hipStream_t stream) {
    const u16 *x = (const u16*)d_in[0], *mem0 = (const u16*)d_in[1], *Wq = (const u16*)d_in[2], *Wk = (const u16*)d_in[3], *Wv = (const u16*)d_in[4], *Wo = (const u16*)d_in[5];
    const u16 *w1 = (const u16*)d_in[8], *b1 = (const u16*)d_in[9], *w2 = (const u16*)d_in[10], *b2 = (const u16*)d_in[11];
    char* ws = (char*)d_ws;
    (void)hipFuncSetAttribute((const void*)k_pre, hipFuncAttributeMaxDynamicSharedMemorySize, 81920);
    (void)hipFuncSetAttribute((const void*)k_main, hipFuncAttributeMaxDynamicSharedMemorySize, 163840);
    k_pre<<<PRE_FOLD + PRE_LN + PRE_INIT + 1, 512, 81920, stream>>>(x, mem0, Wv, Wo, ws);
    k_main<<<256, 512, 163840, stream>>>(x, Wq, Wk, Wv, Wo, w1, b1, w2, b2, (u16*)d_out, ws);
}
```
